```python
import torch

def _softmax(x, axis=-1):
    return torch.softmax(x, dim=axis)
def _gelu(x):
    return torch.nn.functional.gelu(x, approximate="tanh")
def _silu(x):
    return torch.nn.functional.silu(x)
def _sigmoid(x):
    return torch.sigmoid(x)
def _layernorm(x, axis=-1, eps=1e-5):
    m = x.mean(dim=axis, keepdim=True)
    v = x.var(dim=axis, keepdim=True, unbiased=False)
    return (x - m) / torch.sqrt(v + eps)
def _scatter_add(tgt, idx, src, axis=0):
    return tgt.clone().index_add_(axis, idx, src)
def _segment_sum(src, seg, n):
    out = torch.zeros((n,) + src.shape[1:], dtype=src.dtype)
    return out.index_add_(0, seg, src)
def _pad(x, pw):
    flat = tuple(v for pair in reversed(pw) for v in pair)
    return torch.nn.functional.pad(x, flat)

kernel_name = 'interleave_layers__gqa_block_cached__glu_gated_swiglu__decode'

def setup_inputs():

    _w = (lambda a: (lambda t: t.to(torch.bfloat16) if t.is_floating_point() else t)(torch.as_tensor(a)))
    def _A_setup():
        B, Sq, D = 4, 1, 576
        Hq, Hkv, Sk, Dh = 32, 16, 4096, 128
        assert Hq % Hkv == 0, (Hq, Hkv)
        return {
            "x": _w((lambda *s: torch.randn(*s, dtype=torch.bfloat16) * 0.05)(B, Sq, D)),
            "k_cache": _w((lambda *s: torch.randn(*s, dtype=torch.bfloat16) * 0.8)(B, Hkv, Sk, Dh)),
            "v_cache": _w((lambda *s: torch.randn(*s, dtype=torch.bfloat16) * 0.8)(B, Hkv, Sk, Dh)),
            "wq": _w((lambda *s: torch.randn(*s, dtype=torch.bfloat16) * 0.8)(D, Hq * Dh) * (D ** -0.5)),
            "wo": _w((lambda *s: torch.randn(*s, dtype=torch.bfloat16) * 0.8)(Hq * Dh, D) * ((Hq * Dh) ** -0.5)),
        }
    _A_in = _A_setup()

    def _B_setup():
        B, S, D, F = 4, 1, 576, 4608
        return {
            "x": _w((lambda *s: torch.randn(*s, dtype=torch.bfloat16) * 0.05)(B, S, D)),
            "w1": _w((lambda *s: torch.randn(*s, dtype=torch.bfloat16) * 0.2)(D, F) * (2.0 / D) ** 0.5),
            "w3": _w((lambda *s: torch.randn(*s, dtype=torch.bfloat16) * 0.2)(D, F) * (2.0 / D) ** 0.5),
            "w2": _w((lambda *s: torch.randn(*s, dtype=torch.bfloat16) * 0.2)(F, D) * (2.0 / F) ** 0.5),
        }
    _B_in = _B_setup()

    return {
        "A_x": _A_in["x"],
        "A_k_cache": _A_in["k_cache"],
        "A_v_cache": _A_in["v_cache"],
        "A_wq": _A_in["wq"],
        "A_wo": _A_in["wo"],
        "B_x": _B_in["x"],
        "B_w1": _B_in["w1"],
        "B_w3": _B_in["w3"],
        "B_w2": _B_in["w2"],
    }

def reference(A_x, A_k_cache, A_v_cache, A_wq, A_wo, B_x, B_w1, B_w3, B_w2):
    def _A_ref(x, k_cache, v_cache, wq, wo):
        B, Sq, D = x.shape
        Hq, Dh = 32, 128
        Hkv = k_cache.shape[1]
        rep = Hq // Hkv
        q = (lambda x, *d: x.permute(*d))((x @ wq).reshape(B, Sq, Hq, Dh), 0, 2, 1, 3)
        k = (lambda x, n, axis: torch.repeat_interleave(x, n, dim=axis))(k_cache, rep, axis=1)
        v = (lambda x, n, axis: torch.repeat_interleave(x, n, dim=axis))(v_cache, rep, axis=1)
        scores = torch.einsum("bhqd,bhkd->bhqk", q, k) * (Dh ** -0.5)
        p = _softmax(scores, axis=-1)
        o = torch.einsum("bhqk,bhkd->bhqd", p, v)
        return (lambda x, *d: x.permute(*d))(o, 0, 2, 1, 3).reshape(B, Sq, Hq * Dh) @ wo

    def _B_ref(x, w1, w3, w2):
        gate = torch.einsum("bsd,df->bsf", x, w1)
        up = torch.einsum("bsd,df->bsf", x, w3)
        h = _silu(gate) * up
        out = torch.einsum("bsf,fd->bsd", h, w2)
        return out

    x = A_x
    x = x + _A_ref(_layernorm(x, axis=-1), A_k_cache, A_v_cache, A_wq, A_wo)
    x = x + _B_ref(_layernorm(x, axis=-1), B_w1, B_w3, B_w2)
    x = x + _A_ref(_layernorm(x, axis=-1), A_k_cache, A_v_cache, A_wq, A_wo)
    x = x + _B_ref(_layernorm(x, axis=-1), B_w1, B_w3, B_w2)
    return x
```

```cpp
#include <hip/hip_runtime.h>
#include <hip/hip_bf16.h>
#include <stdint.h>

namespace {
constexpr int B = 4, D = 576, HQ = 32, HKV = 16, SK = 4096, DH = 128, F = 4608;
constexpr int NSPLIT = 8;
typedef unsigned short u16;
typedef unsigned v4u __attribute__((ext_vector_type(4)));
__device__ __forceinline__ float bf2f(u16 b) { return __builtin_bit_cast(float, (unsigned)b << 16); }
__device__ __forceinline__ u16 f2bf(float f) {
  unsigned u = __builtin_bit_cast(unsigned, f);
  if ((u & 0x7fffffffu) > 0x7f800000u) return (u16)((u >> 16) | 0x40);
  u += 0x7FFFu + ((u >> 16) & 1u);
  return (u16)(u >> 16);
}
__device__ __forceinline__ float rbf(float f) { return bf2f(f2bf(f)); }
__device__ __forceinline__ float wave_sum(float v) {
  for (int o = 32; o > 0; o >>= 1) v += __shfl_xor(v, o);
  return v;
}
__device__ __forceinline__ float wave_max(float v) {
  for (int o = 32; o > 0; o >>= 1) v = fmaxf(v, __shfl_xor(v, o));
  return v;
}
__device__ void ln_to_lds(const u16* x, float* xn) {
  const int w = threadIdx.x >> 6, lane = threadIdx.x & 63;
  float v[9], s = 0.f;
  for (int i = 0; i < 9; ++i) { v[i] = bf2f(x[w * D + lane + 64 * i]); s += v[i]; }
  s = wave_sum(s);
  const float mean = s * (1.f / D);
  float q = 0.f;
  for (int i = 0; i < 9; ++i) { float d = v[i] - mean; q += d * d; }
  q = wave_sum(q);
  const float var = rbf(q * (1.f / D)), mb = rbf(mean);
  const float den = rbf(sqrtf(rbf(var + 1e-5f)));
  for (int i = 0; i < 9; ++i) xn[w * D + lane + 64 * i] = rbf(rbf(v[i] - mb) / den);
}
template <int N>
__device__ void gemv16(const float* xn, const u16* W, int n0, float* red, float* res) {
  const int c = threadIdx.x & 15, ks = threadIdx.x >> 4;
  float a[4] = {0, 0, 0, 0};
  for (int k = ks; k < D; k += 16) {
    const float w = bf2f(W[(size_t)k * N + n0 + c]);
    for (int b = 0; b < 4; ++b) a[b] += xn[b * D + k] * w;
  }
  for (int b = 0; b < 4; ++b) red[(ks * 4 + b) * 16 + c] = a[b];
  __syncthreads();
  if (threadIdx.x < 64) {
    float s = 0.f;
    for (int j = 0; j < 16; ++j) s += red[j * 64 + threadIdx.x];
    res[threadIdx.x] = s;
  }
  __syncthreads();
}
__global__ void __launch_bounds__(256) k_q(const u16* x, const u16* wq, float* q) {
  __shared__ float xn[4 * D], red[16 * 64], res[64];
  ln_to_lds(x, xn);
  __syncthreads();
  gemv16<HQ * DH>(xn, wq, blockIdx.x * 16, red, res);
  if (threadIdx.x < 64) q[(threadIdx.x >> 4) * (HQ * DH) + blockIdx.x * 16 + (threadIdx.x & 15)] = rbf(res[threadIdx.x]);
}
__global__ void __launch_bounds__(256) k_attn(const float* q, const u16* kc, const u16* vc, float* part) {
  constexpr int NK = SK / NSPLIT;
  __shared__ float sc[2][NK], red[8], ored[16][2][DH];
  const int s = blockIdx.x, kvh = blockIdx.y, b = blockIdx.z;
  const int tid = threadIdx.x, c = tid & 15, r = tid >> 4;
  const u16* K = kc + ((size_t)(b * HKV + kvh) * SK + s * NK) * DH;
  const u16* V = vc + ((size_t)(b * HKV + kvh) * SK + s * NK) * DH;
  float q0[8], q1[8];
  for (int j = 0; j < 8; ++j) { q0[j] = q[b * HQ * DH + (2 * kvh) * DH + 8 * c + j]; q1[j] = q[b * HQ * DH + (2 * kvh + 1) * DH + 8 * c + j]; }
#pragma unroll 4
  for (int k = r; k < NK; k += 16) {
    v4u kv = __builtin_nontemporal_load((const v4u*)(K + (size_t)k * DH + 8 * c));
    float d0 = 0.f, d1 = 0.f;
    for (int j = 0; j < 4; ++j) {
      float lo = __builtin_bit_cast(float, kv[j] << 16), hi = __builtin_bit_cast(float, kv[j] & 0xffff0000u);
      d0 += lo * q0[2 * j] + hi * q0[2 * j + 1];
      d1 += lo * q1[2 * j] + hi * q1[2 * j + 1];
    }
    for (int o = 8; o > 0; o >>= 1) { d0 += __shfl_xor(d0, o); d1 += __shfl_xor(d1, o); }
    if (c == 0) { sc[0][k] = rbf(rbf(d0) * 0.08838834764831845f); sc[1][k] = rbf(rbf(d1) * 0.08838834764831845f); }
  }
  __syncthreads();
  float m0 = -INFINITY, m1 = -INFINITY;
  for (int k = tid; k < NK; k += 256) { m0 = fmaxf(m0, sc[0][k]); m1 = fmaxf(m1, sc[1][k]); }
  m0 = wave_max(m0); m1 = wave_max(m1);
  if ((tid & 63) == 0) { red[tid >> 6] = m0; red[4 + (tid >> 6)] = m1; }
  __syncthreads();
  m0 = fmaxf(fmaxf(red[0], red[1]), fmaxf(red[2], red[3]));
  m1 = fmaxf(fmaxf(red[4], red[5]), fmaxf(red[6], red[7]));
  __syncthreads();
  float l0 = 0.f, l1 = 0.f;
  for (int k = tid; k < NK; k += 256) { float p0 = __expf(sc[0][k] - m0), p1 = __expf(sc[1][k] - m1); sc[0][k] = p0; sc[1][k] = p1; l0 += p0; l1 += p1; }
  l0 = wave_sum(l0); l1 = wave_sum(l1);
  if ((tid & 63) == 0) { red[tid >> 6] = l0; red[4 + (tid >> 6)] = l1; }
  __syncthreads();
  l0 = red[0] + red[1] + red[2] + red[3];
  l1 = red[4] + red[5] + red[6] + red[7];
  float a0[8] = {0}, a1[8] = {0};
#pragma unroll 4
  for (int k = r; k < NK; k += 16) {
    v4u vv = __builtin_nontemporal_load((const v4u*)(V + (size_t)k * DH + 8 * c));
    const float p0 = sc[0][k], p1 = sc[1][k];
    for (int j = 0; j < 4; ++j) {
      float lo = __builtin_bit_cast(float, vv[j] << 16), hi = __builtin_bit_cast(float, vv[j] & 0xffff0000u);
      a0[2 * j] += p0 * lo; a0[2 * j + 1] += p0 * hi;
      a1[2 * j] += p1 * lo; a1[2 * j + 1] += p1 * hi;
    }
  }
  for (int j = 0; j < 8; ++j) { ored[r][0][8 * c + j] = a0[j]; ored[r][1][8 * c + j] = a1[j]; }
  __syncthreads();
  {
    const int h = tid >> 7, d = tid & 127;
    float o = 0.f;
    for (int j = 0; j < 16; ++j) o += ored[j][h][d];
    float* P = part + ((size_t)((b * HQ + 2 * kvh + h) * NSPLIT + s)) * 132;
    P[d] = o;
    if (d == 0) { P[128] = h ? m1 : m0; P[129] = h ? l1 : l0; }
  }
}
__global__ void __launch_bounds__(128) k_comb(const float* part, float* o) {
  const float* P = part + (size_t)blockIdx.x * NSPLIT * 132;
  float M = -INFINITY;
  for (int s = 0; s < NSPLIT; ++s) M = fmaxf(M, P[s * 132 + 128]);
  float L = 0.f, a = 0.f;
  for (int s = 0; s < NSPLIT; ++s) { float w = __expf(P[s * 132 + 128] - M); L += w * P[s * 132 + 129]; a += w * P[s * 132 + threadIdx.x]; }
  o[blockIdx.x * 128 + threadIdx.x] = rbf(a / L);
}
template <int K>
__global__ void __launch_bounds__(256) k_proj_res(const float* a, const u16* W, const u16* xin, u16* xout) {
  __shared__ float red[32 * 32];
  const int c = threadIdx.x & 7, ks = threadIdx.x >> 3, n0 = blockIdx.x * 8;
  float acc[4] = {0, 0, 0, 0};
#pragma unroll 8
  for (int k = ks; k < K; k += 32) {
    const float w = bf2f(W[(size_t)k * D + n0 + c]);
    for (int b = 0; b < 4; ++b) acc[b] += a[b * K + k] * w;
  }
  for (int b = 0; b < 4; ++b) red[ks * 32 + b * 8 + c] = acc[b];
  __syncthreads();
  if (threadIdx.x < 32) {
    float s = 0.f;
    for (int j = 0; j < 32; ++j) s += red[j * 32 + threadIdx.x];
    const int b = threadIdx.x >> 3, n = n0 + (threadIdx.x & 7);
    xout[b * D + n] = f2bf(bf2f(xin[b * D + n]) + rbf(s));
  }
}
__global__ void __launch_bounds__(256) k_gu(const u16* x, const u16* w1, const u16* w3, float* h) {
  __shared__ float xn[4 * D], red[16 * 64], rg[64], ru[64];
  ln_to_lds(x, xn);
  __syncthreads();
  gemv16<F>(xn, w1, blockIdx.x * 16, red, rg);
  gemv16<F>(xn, w3, blockIdx.x * 16, red, ru);
  if (threadIdx.x < 64) {
    const float g = rbf(rg[threadIdx.x]), u = rbf(ru[threadIdx.x]);
    const float sl = rbf(g / (1.f + __expf(-g)));
    h[(threadIdx.x >> 4) * F + blockIdx.x * 16 + (threadIdx.x & 15)] = rbf(sl * u);
  }
}
}

extern "C" void kernel_launch(void* const* d_in, const int* in_sizes, int n_in, void* d_out, int out_size, void* d_ws, size_t ws_size,
                              hipStream_t stream) {
  const u16* x0 = (const u16*)d_in[0];
  const u16 *kc = (const u16*)d_in[1], *vc = (const u16*)d_in[2], *wq = (const u16*)d_in[3], *wo = (const u16*)d_in[4];
  const u16 *w1 = (const u16*)d_in[6], *w3 = (const u16*)d_in[7], *w2 = (const u16*)d_in[8];
  char* ws = (char*)d_ws;
  float* q = (float*)ws;
  float* o = (float*)(ws + 65536);
  float* h = (float*)(ws + 2 * 65536);
  u16* xa = (u16*)(ws + 4 * 65536);
  u16* xb = (u16*)(ws + 5 * 65536);
  float* part = (float*)(ws + 6 * 65536);
  const u16* xin = x0;
  for (int l = 0; l < 2; ++l) {
    k_q<<<HQ * DH / 16, 256, 0, stream>>>(xin, wq, q);
    k_attn<<<dim3(NSPLIT, HKV, B), 256, 0, stream>>>(q, kc, vc, part);
    k_comb<<<B * HQ, 128, 0, stream>>>(part, o);
    k_proj_res<HQ * DH><<<D / 8, 256, 0, stream>>>(o, wo, xin, xa);
    k_gu<<<F / 16, 256, 0, stream>>>(xa, w1, w3, h);
    u16* xo = l == 1 ? (u16*)d_out : xb;
    k_proj_res<F><<<D / 8, 256, 0, stream>>>(h, w2, xa, xo);
    xin = xb;
  }
}
```

```cpp
#include <hip/hip_runtime.h>
#include <stdint.h>

__global__ void __launch_bounds__(256, 1)
fused_decode(const void* x, const void* k, const void* v, const void* wq, const void* wo, const void* w1, const void* w3,
             const void* w2, void* out, void* ws) {
  auto ka = __builtin_amdgcn_kernarg_segment_ptr();
  unsigned wg = blockIdx.x;
  unsigned tid = threadIdx.x;
  asm volatile(
      "s_load_dwordx16 s[64:79], s[4:5], 0\n"
      "s_load_dwordx4 s[60:63], s[4:5], 64\n"
      "v_lshrrev_b32 v1, 6, v0\n"
      "v_and_b32 v0, 63, v0\n"
      "v_readfirstlane_b32 s44, v1\n"
      "s_and_b32 s0, s6, 7\n"
      "s_lshr_b32 s1, s6, 3\n"
      "s_and_b32 s2, s1, 1\n"
      "s_lshl3_add_u32 s45, s2, s0\n"
      "s_lshr_b32 s46, s6, 4\n"
      "s_lshr_b32 s7, s46, 3\n"
      "s_lshl2_add_u32 s52, s6, s44\n"
      "s_mul_i32 s52, s52, 5\n"
      "s_add_u32 s52, s52, s1\n"
      "s_lshl_b32 s59, s44, 14\n"
      "s_mov_b32 s53, 0\n"
      "s_mov_b32 s49, 110592\n"
      "s_mov_b32 s56, 0x05040100\n"
      "s_mov_b32 s57, 0x07060302\n"
      "s_mov_b32 s58, 0x3f803f80\n"
      "s_sub_u32 s2, s1, 18\n"
      "s_lshl3_add_u32 s48, s2, s0\n"
      "s_cmp_lt_u32 s1, 27\n"
      "s_cselect_b32 s47, 1, 2\n"
      "s_mul_i32 s2, s0, 18\n"
      "s_add_u32 s2, s2, s1\n"
      "s_cmp_lt_u32 s1, 18\n"
      "s_cselect_b32 s48, s2, s48\n"
      "s_cselect_b32 s47, 0, s47\n"
      "s_cmp_eq_u32 s47, 2\n"
      "s_cselect_b32 s48, 0, s48\n"
      "s_lshr_b32 s2, s46, 2\n"
      "s_lshl4_add_u32 s2, s2, s45\n"
      "s_and_b32 s3, s46, 3\n"
      "s_lshl2_add_u32 s2, s2, s3\n"
      "s_lshl2_add_u32 s2, s2, s44\n"
      "s_lshl_b32 s3, s2, 16\n"
      "v_and_b32 v63, 15, v0\n"
      "v_lshlrev_b32 v63, 8, v63\n"
      "v_and_or_b32 v63, v0, 48, v63\n"
      "v_lshlrev_b32 v62, 4, v0\n"
      "v_mov_b32 v56, -1\n"
      "v_mov_b32 v57, -1\n"
      "v_mov_b32 v58, -1\n"
      "v_mov_b32 v59, -1\n"
      "v_lshlrev_b32 v60, 3, v0\n"
      "s_waitcnt lgkmcnt(0)\n"
      "s_load_dwordx2 s[50:51], s[62:63], 0\n"
      "s_mov_b32 s16, s62\n"
      "s_mov_b32 s17, s63\n"
      "s_mov_b32 s18, 1613824\n"
      "s_mov_b32 s19, 0x20000\n"
      "s_add_u32 s8, s66, s3\n"
      "s_addc_u32 s9, s67, 0\n"
      "s_mov_b32 s10, 65536\n"
      "s_mov_b32 s11, 0x20000\n"
      "s_add_u32 s12, s68, s3\n"
      "s_addc_u32 s13, s69, 0\n"
      "s_mov_b32 s14, 65536\n"
      "s_mov_b32 s15, 0x20000\n"
      "s_lshl2_add_u32 s0, s6, s44\n"
      "s_mul_i32 s0, s0, 1536\n"
      "s_add_u32 s0, s0, 40960\n"
      "buffer_store_dwordx4 v[56:59], v62, s[16:19], s0 offen sc1\n"
      "buffer_store_dwordx2 v[56:57], v60, s[16:19], s0 offen offset:1024 sc1\n"
      "s_mov_b32 s36, s64\n"
      "s_mov_b32 s37, s65\n"
      "s_mov_b32 s38, 4608\n"
      "s_mov_b32 s39, 0x20000\n"
      "s_lshl_b32 s0, s45, 9\n"
      "s_lshl_b32 s1, s46, 5\n"
      "s_add_u32 s0, s0, s1\n"
      "s_sub_u32 s1, 4718592, s0\n"
      "s_add_u32 s20, s70, s0\n"
      "s_addc_u32 s21, s71, 0\n"
      "s_mov_b32 s22, s1\n"
      "s_mov_b32 s23, 0x20000\n"
      "s_cmp_eq_u32 s44, 0\n"
      "s_cbranch_scc0 .Lw0_1_%=\n"
      "v_bfe_u32 v5, v0, 2, 2\n"
      "v_lshrrev_b32 v6, 4, v0\n"
      "v_and_b32 v7, 3, v0\n"
      "v_mad_u32_u24 v1, v5, 18, v6\n"
      "v_lshlrev_b32 v1, 4, v1\n"
      "s_movk_i32 s0, 0x480\n"
      "v_mad_u32_u24 v1, v7, s0, v1\n"
      "v_mov_b32 v2, 0x40000000\n"
      "v_cmp_gt_u32 vcc, 32, v0\n"
      "s_nop 1\n"
      "v_cndmask_b32 v2, v2, v1, vcc\n"
      "v_lshl_add_u32 v3, v6, 3, v5\n"
      "v_lshlrev_b32 v3, 5, v3\n"
      "v_lshl_add_u32 v3, v7, 3, v3\n"
      "v_add_u32 v3, 139264, v3\n"
      "v_and_b32 v4, 2, v6\n"
      "v_lshlrev_b32 v4, 8, v4\n"
      "v_sub_u32 v4, v3, v4\n"
      "buffer_load_dwordx4 v[8:11], v1, s[36:39], 0 offen offset:0\n"
      "buffer_load_dwordx4 v[12:15], v1, s[36:39], 0 offen offset:64\n"
      "buffer_load_dwordx4 v[16:19], v1, s[36:39], 0 offen offset:128\n"
      "buffer_load_dwordx4 v[20:23], v1, s[36:39], 0 offen offset:192\n"
      "buffer_load_dwordx4 v[24:27], v2, s[36:39], 0 offen offset:256\n"
      "s_branch .Lj_2_%=\n"
      ".Lw0_1_%=:\n"
      "v_lshrrev_b32 v1, 1, v0\n"
      "v_lshlrev_b32 v1, 13, v1\n"
      "v_and_b32 v2, 1, v0\n"
      "v_lshl_add_u32 v1, v2, 4, v1\n"
      "s_mul_i32 s1, s44, 6\n"
      "s_sub_u32 s1, s1, 6\n"
      "s_lshl_b32 s0, s1, 18\n"
      "s_lshl_b32 s1, s1, 10\n"
      "s_add_u32 m0, s1, 139264\n"
      "s_nop 0\n"
      "buffer_load_dwordx4 v1, s[20:23], s0 offen nt lds\n"
      "s_add_u32 m0, s1, 140288\n"
      "s_add_u32 s0, s0, 0x40000\n"
      "buffer_load_dwordx4 v1, s[20:23], s0 offen nt lds\n"
      "s_add_u32 m0, s1, 141312\n"
      "s_add_u32 s0, s0, 0x40000\n"
      "buffer_load_dwordx4 v1, s[20:23], s0 offen nt lds\n"
      "s_add_u32 m0, s1, 142336\n"
      "s_add_u32 s0, s0, 0x40000\n"
      "buffer_load_dwordx4 v1, s[20:23], s0 offen nt lds\n"
      "s_add_u32 m0, s1, 143360\n"
      "s_add_u32 s0, s0, 0x40000\n"
      "buffer_load_dwordx4 v1, s[20:23], s0 offen nt lds\n"
      "s_add_u32 m0, s1, 144384\n"
      "s_add_u32 s0, s0, 0x40000\n"
      "buffer_load_dwordx4 v1, s[20:23], s0 offen nt lds\n"
      ".Lj_2_%=:\n"
      "s_add_u32 s0, s52, 0\n"
      "s_and_b32 s0, s0, 15\n"
      "s_lshl_b32 s0, s0, 12\n"
      "buffer_load_dwordx4 a[0:3], v63, s[8:11], s0 offen offset:0 nt\n"
      "buffer_load_dwordx4 a[4:7], v63, s[8:11], s0 offen offset:64 nt\n"
      "buffer_load_dwordx4 a[8:11], v63, s[8:11], s0 offen offset:128 nt\n"
      "buffer_load_dwordx4 a[12:15], v63, s[8:11], s0 offen offset:192 nt\n"
      "s_add_u32 s0, s52, 1\n"
      "s_and_b32 s0, s0, 15\n"
      "s_lshl_b32 s0, s0, 12\n"
      "buffer_load_dwordx4 a[16:19], v63, s[8:11], s0 offen offset:0 nt\n"
      "buffer_load_dwordx4 a[20:23], v63, s[8:11], s0 offen offset:64 nt\n"
      "buffer_load_dwordx4 a[24:27], v63, s[8:11], s0 offen offset:128 nt\n"
      "buffer_load_dwordx4 a[28:31], v63, s[8:11], s0 offen offset:192 nt\n"
      "s_add_u32 s0, s52, 2\n"
      "s_and_b32 s0, s0, 15\n"
      "s_lshl_b32 s0, s0, 12\n"
      "buffer_load_dwordx4 a[32:35], v63, s[8:11], s0 offen offset:0 nt\n"
      "buffer_load_dwordx4 a[36:39], v63, s[8:11], s0 offen offset:64 nt\n"
      "buffer_load_dwordx4 a[40:43], v63, s[8:11], s0 offen offset:128 nt\n"
      "buffer_load_dwordx4 a[44:47], v63, s[8:11], s0 offen offset:192 nt\n"
      "s_add_u32 s0, s52, 0\n"
      "s_and_b32 s0, s0, 15\n"
      "s_lshl_b32 s0, s0, 12\n"
      "buffer_load_dwordx4 v[64:67], v62, s[12:15], s0 offen offset:0 nt\n"
      "buffer_load_dwordx4 v[96:99], v62, s[12:15], s0 offen offset:1024 nt\n"
      "buffer_load_dwordx4 v[80:83], v62, s[12:15], s0 offen offset:2048 nt\n"
      "buffer_load_dwordx4 v[112:115], v62, s[12:15], s0 offen offset:3072 nt\n"
      "s_mov_b32 s40, s60\n"
      "s_mov_b32 s41, s61\n"
      "s_mov_b32 s42, 4608\n"
      "s_mov_b32 s43, 0x20000\n"
      "s_lshr_b32 s60, s46, 2\n"
      "s_lshl1_add_u32 s0, s45, s7\n"
      "s_lshl2_add_u32 s0, s0, s44\n"
      "s_mul_i32 s0, s0, 36864\n"
      "s_and_b32 s1, s46, 7\n"
      "s_mul_i32 s1, s1, 144\n"
      "s_add_u32 s0, s0, s1\n"
      "s_add_u32 s24, s72, s0\n"
      "s_addc_u32 s25, s73, 0\n"
      "s_mov_b32 s26, 35856\n"
      "s_mov_b32 s27, 0x20000\n"
      "s_add_u32 s0, s52, 3\n"
      "s_and_b32 s0, s0, 15\n"
      "s_lshl_b32 s0, s0, 12\n"
      "buffer_load_dwordx4 a[48:51], v63, s[8:11], s0 offen offset:0 nt\n"
      "buffer_load_dwordx4 a[52:55], v63, s[8:11], s0 offen offset:64 nt\n"
      "buffer_load_dwordx4 a[56:59], v63, s[8:11], s0 offen offset:128 nt\n"
      "buffer_load_dwordx4 a[60:63], v63, s[8:11], s0 offen offset:192 nt\n"
      "s_add_u32 s0, s52, 1\n"
      "s_and_b32 s0, s0, 15\n"
      "s_lshl_b32 s0, s0, 12\n"
      "buffer_load_dwordx4 v[72:75], v62, s[12:15], s0 offen offset:0 nt\n"
      "buffer_load_dwordx4 v[104:107], v62, s[12:15], s0 offen offset:1024 nt\n"
      "buffer_load_dwordx4 v[88:91], v62, s[12:15], s0 offen offset:2048 nt\n"
      "buffer_load_dwordx4 v[120:123], v62, s[12:15], s0 offen offset:3072 nt\n"
      "s_bitcmp1_b32 s48, 0\n"
      "s_cselect_b64 s[74:75], s[76:77], s[74:75]\n"
      "s_and_b32 s0, s48, -2\n"
      "s_and_b32 s34, s44, 3\n"
      "s_lshr_b32 s1, s48, 3\n"
      "s_and_b32 s2, s48, 7\n"
      "s_mul_i32 s2, s2, 5184\n"
      "s_add_u32 s1, s1, s2\n"
      "s_lshl_b32 s1, s1, 1\n"
      "s_movk_i32 s2, 1152\n"
      "s_cmp_eq_u32 s47, 0\n"
      "s_cselect_b64 s[74:75], s[74:75], s[78:79]\n"
      "s_cselect_b32 s0, s0, s1\n"
      "s_cselect_b32 s2, 9216, s2\n"
      "s_cselect_b32 s34, s34, s44\n"
      "s_lshl_b32 s0, s0, 6\n"
      "s_sub_u32 s1, 5308416, s0\n"
      "s_cmp_eq_u32 s47, 2\n"
      "s_cselect_b32 s1, 0, s1\n"
      "s_cselect_b32 s2, 0, s2\n"
      "s_lshl_b32 s33, s2, 5\n"
      "s_mul_i32 s34, s2, s34\n"
      "s_lshl_b32 s34, s34, 3\n"
      "s_lshl_b32 s35, s44, 10\n"
      "s_add_u32 s35, s35, 65536\n"
      "s_mov_b32 s32, 120832\n"
      "s_add_u32 s28, s74, s0\n"
      "s_addc_u32 s29, s75, 0\n"
      "s_mov_b32 s30, s1\n"
      "s_mov_b32 s31, 0x20000\n"
      "s_add_u32 s0, s52, 4\n"
      "s_and_b32 s0, s0, 15\n"
      "s_lshl_b32 s0, s0, 12\n"
      "buffer_load_dwordx4 a[64:67], v63, s[8:11], s0 offen offset:0 nt\n"
      "buffer_load_dwordx4 a[68:71], v63, s[8:11], s0 offen offset:64 nt\n"
      "buffer_load_dwordx4 a[72:75], v63, s[8:11], s0 offen offset:128 nt\n"
      "buffer_load_dwordx4 a[76:79], v63, s[8:11], s0 offen offset:192 nt\n"
      "s_add_u32 s0, s52, 2\n"
      "s_and_b32 s0, s0, 15\n"
      "s_lshl_b32 s0, s0, 12\n"
      "buffer_load_dwordx4 v[68:71], v62, s[12:15], s0 offen offset:0 nt\n"
      "buffer_load_dwordx4 v[100:103], v62, s[12:15], s0 offen offset:1024 nt\n"
      "buffer_load_dwordx4 v[84:87], v62, s[12:15], s0 offen offset:2048 nt\n"
      "buffer_load_dwordx4 v[116:119], v62, s[12:15], s0 offen offset:3072 nt\n"
      "s_waitcnt vmcnt(32)\n"
      "s_waitcnt lgkmcnt(0)\n"
      "s_add_u32 s50, s50, 1\n"
      "s_addc_u32 s51, s51, 0\n"
      "s_barrier\n"
      "s_cmp_eq_u32 s44, 0\n"
      "s_cbranch_scc0 .Lw0_3_%=\n"
      "ds_read_b64_tr_b16 v[48:49], v3 offset:0\n"
      "ds_read_b64_tr_b16 v[50:51], v3 offset:128\n"
      "ds_read_b64_tr_b16 v[52:53], v3 offset:4608\n"
      "ds_read_b64_tr_b16 v[54:55], v3 offset:4736\n"
      "ds_read_b64_tr_b16 v[56:57], v3 offset:9216\n"
      "ds_read_b64_tr_b16 v[58:59], v3 offset:9344\n"
      "ds_read_b64_tr_b16 v[60:61], v3 offset:13824\n"
      "ds_read_b64_tr_b16 v[62:63], v3 offset:13952\n"
      "ds_read_b64_tr_b16 v[36:37], v3 offset:1024\n"
      "ds_read_b64_tr_b16 v[38:39], v3 offset:1152\n"
      "ds_read_b64_tr_b16 v[40:41], v3 offset:5632\n"
      "ds_read_b64_tr_b16 v[42:43], v3 offset:5760\n"
      "v_mov_b32 v7, 0\n"
      "v_cmp_gt_u32 vcc, 32, v0\n"
      "v_mov_b32 v28, s58\n"
      "v_mov_b32 v29, s58\n"
      "v_mov_b32 v30, s58\n"
      "v_mov_b32 v31, s58\n"
      "v_cndmask_b32 v32, v7, v28, vcc\n"
      "v_cndmask_b32 v33, v7, v28, vcc\n"
      "v_cndmask_b32 v34, v7, v28, vcc\n"
      "v_cndmask_b32 v35, v7, v28, vcc\n"
      "s_waitcnt lgkmcnt(10)\n"
      "v_mfma_f32_16x16x32_bf16 v[44:47], v[48:51], v[28:31], 0\n"
      "ds_read_b64_tr_b16 v[48:49], v3 offset:10240\n"
      "ds_read_b64_tr_b16 v[50:51], v3 offset:10368\n"
      "s_waitcnt lgkmcnt(10)\n"
      "v_mfma_f32_16x16x32_bf16 v[44:47], v[52:55], v[28:31], v[44:47]\n"
      "ds_read_b64_tr_b16 v[52:53], v3 offset:14848\n"
      "ds_read_b64_tr_b16 v[54:55], v3 offset:14976\n"
      "s_waitcnt lgkmcnt(10)\n"
      "v_mfma_f32_16x16x32_bf16 v[44:47], v[56:59], v[28:31], v[44:47]\n"
      "ds_read_b64_tr_b16 v[56:57], v3 offset:2048\n"
      "ds_read_b64_tr_b16 v[58:59], v3 offset:2176\n"
      "s_waitcnt lgkmcnt(10)\n"
      "v_mfma_f32_16x16x32_bf16 v[44:47], v[60:63], v[28:31], v[44:47]\n"
      "ds_read_b64_tr_b16 v[60:61], v3 offset:6656\n"
      "ds_read_b64_tr_b16 v[62:63], v3 offset:6784\n"
      "s_waitcnt lgkmcnt(10)\n"
      "v_mfma_f32_16x16x32_bf16 v[44:47], v[36:39], v[28:31], v[44:47]\n"
      "ds_read_b64_tr_b16 v[36:37], v3 offset:11264\n"
      "ds_read_b64_tr_b16 v[38:39], v3 offset:11392\n"
      "s_waitcnt lgkmcnt(10)\n"
      "v_mfma_f32_16x16x32_bf16 v[44:47], v[40:43], v[28:31], v[44:47]\n"
      "ds_read_b64_tr_b16 v[40:41], v3 offset:15872\n"
      "ds_read_b64_tr_b16 v[42:43], v3 offset:16000\n"
      "s_waitcnt lgkmcnt(10)\n"
      "v_mfma_f32_16x16x32_bf16 v[44:47], v[48:51], v[28:31], v[44:47]\n"
      "ds_read_b64_tr_b16 v[48:49], v3 offset:3072\n"
      "ds_read_b64_tr_b16 v[50:51], v3 offset:3200\n"
      "s_waitcnt lgkmcnt(10)\n"
      "v_mfma_f32_16x16x32_bf16 v[44:47], v[52:55], v[28:31], v[44:47]\n"
      "ds_read_b64_tr_b16 v[52:53], v3 offset:7680\n"
      "ds_read_b64_tr_b16 v[54:55], v3 offset:7808\n"
      "s_waitcnt lgkmcnt(10)\n"
      "v_mfma_f32_16x16x32_bf16 v[44:47], v[56:59], v[28:31], v[44:47]\n"
      "ds_read_b64_tr_b16 v[56:57], v3 offset:12288\n"
      "ds_read_b64_tr_b16 v[58:59], v3 offset:12416\n"
      "s_waitcnt lgkmcnt(10)\n"
      "v_mfma_f32_16x16x32_bf16 v[44:47], v[60:63], v[28:31], v[44:47]\n"
      "ds_read_b64_tr_b16 v[60:61], v3 offset:16896\n"
      "ds_read_b64_tr_b16 v[62:63], v3 offset:17024\n"
      "s_waitcnt lgkmcnt(10)\n"
      "v_mfma_f32_16x16x32_bf16 v[44:47], v[36:39], v[28:31], v[44:47]\n"
      "ds_read_b64_tr_b16 v[36:37], v4 offset:4096\n"
      "ds_read_b64_tr_b16 v[38:39], v4 offset:4224\n"
      "s_waitcnt lgkmcnt(10)\n"
      "v_mfma_f32_16x16x32_bf16 v[44:47], v[40:43], v[28:31], v[44:47]\n"
      "ds_read_b64_tr_b16 v[40:41], v4 offset:8704\n"
      "ds_read_b64_tr_b16 v[42:43], v4 offset:8832\n"
      "s_waitcnt lgkmcnt(10)\n"
      "v_mfma_f32_16x16x32_bf16 v[44:47], v[48:51], v[28:31], v[44:47]\n"
      "ds_read_b64_tr_b16 v[48:49], v4 offset:13312\n"
      "ds_read_b64_tr_b16 v[50:51], v4 offset:13440\n"
      "s_waitcnt lgkmcnt(10)\n"
      "v_mfma_f32_16x16x32_bf16 v[44:47], v[52:55], v[28:31], v[44:47]\n"
      "ds_read_b64_tr_b16 v[52:53], v4 offset:17920\n"
      "ds_read_b64_tr_b16 v[54:55], v4 offset:18048\n"
      "s_waitcnt lgkmcnt(10)\n"
      "v_mfma_f32_16x16x32_bf16 v[44:47], v[56:59], v[28:31], v[44:47]\n"
      "s_waitcnt lgkmcnt(8)\n"
      "v_mfma_f32_16x16x32_bf16 v[44:47], v[60:63], v[28:31], v[44:47]\n"
      "s_waitcnt lgkmcnt(6)\n"
      "v_mfma_f32_16x16x32_bf16 v[44:47], v[36:39], v[32:35], v[44:47]\n"
      "s_waitcnt lgkmcnt(4)\n"
      "v_mfma_f32_16x16x32_bf16 v[44:47], v[40:43], v[32:35], v[44:47]\n"
      "s_waitcnt lgkmcnt(2)\n"
      "v_mfma_f32_16x16x32_bf16 v[44:47], v[48:51], v[32:35], v[44:47]\n"
      "s_waitcnt lgkmcnt(0)\n"
      "v_mfma_f32_16x16x32_bf16 v[44:47], v[52:55], v[32:35], v[44:47]\n"
      "v_and_b32 v7, 3, v0\n"
      "v_lshlrev_b32 v7, 5, v7\n"
      "v_and_or_b32 v7, v0, 16, v7\n"
      "ds_read_b64_tr_b16 v[48:49], v3 offset:0\n"
      "ds_read_b64_tr_b16 v[50:51], v3 offset:128\n"
      "ds_read_b64_tr_b16 v[52:53], v3 offset:4608\n"
      "ds_read_b64_tr_b16 v[54:55], v3 offset:4736\n"
      "ds_read_b64_tr_b16 v[56:57], v3 offset:9216\n"
      "ds_read_b64_tr_b16 v[58:59], v3 offset:9344\n"
      "ds_read_b64_tr_b16 v[60:61], v3 offset:13824\n"
      "ds_read_b64_tr_b16 v[62:63], v3 offset:13952\n"
      "v_dot2_f32_bf16 v5, v8, s58, 0\n"
      "v_dot2_f32_bf16 v6, v8, v8, 0\n"
      "v_dot2_f32_bf16 v5, v9, s58, v5\n"
      "v_dot2_f32_bf16 v6, v9, v9, v6\n"
      "v_dot2_f32_bf16 v5, v10, s58, v5\n"
      "v_dot2_f32_bf16 v6, v10, v10, v6\n"
      "v_dot2_f32_bf16 v5, v11, s58, v5\n"
      "v_dot2_f32_bf16 v6, v11, v11, v6\n"
      "s_waitcnt lgkmcnt(6)\n"
      "v_mfma_f32_16x16x32_bf16 v[28:31], v[48:51], v[8:11], 0\n"
      "v_dot2_f32_bf16 v5, v12, s58, v5\n"
      "v_dot2_f32_bf16 v6, v12, v12, v6\n"
      "ds_read_b64_tr_b16 v[48:49], v3 offset:1024\n"
      "ds_read_b64_tr_b16 v[50:51], v3 offset:1152\n"
      "s_waitcnt lgkmcnt(6)\n"
      "v_mfma_f32_16x16x32_bf16 v[32:35], v[52:55], v[8:11], 0\n"
      "v_dot2_f32_bf16 v5, v13, s58, v5\n"
      "v_dot2_f32_bf16 v6, v13, v13, v6\n"
      "ds_read_b64_tr_b16 v[52:53], v3 offset:5632\n"
      "ds_read_b64_tr_b16 v[54:55], v3 offset:5760\n"
      "s_waitcnt lgkmcnt(6)\n"
      "v_mfma_f32_16x16x32_bf16 v[36:39], v[56:59], v[8:11], 0\n"
      "v_dot2_f32_bf16 v5, v14, s58, v5\n"
      "v_dot2_f32_bf16 v6, v14, v14, v6\n"
      "ds_read_b64_tr_b16 v[56:57], v3 offset:10240\n"
      "ds_read_b64_tr_b16 v[58:59], v3 offset:10368\n"
      "s_waitcnt lgkmcnt(6)\n"
      "v_mfma_f32_16x16x32_bf16 v[40:43], v[60:63], v[8:11], 0\n"
      "v_dot2_f32_bf16 v5, v15, s58, v5\n"
      "v_dot2_f32_bf16 v6, v15, v15, v6\n"
      "ds_read_b64_tr_b16 v[60:61], v3 offset:14848\n"
      "ds_read_b64_tr_b16 v[62:63], v3 offset:14976\n"
      "ds_read_b64_tr_b16 v[8:9], v3 offset:2048\n"
      "ds_read_b64_tr_b16 v[10:11], v3 offset:2176\n"
      "s_waitcnt lgkmcnt(8)\n"
      "v_mfma_f32_16x16x32_bf16 v[28:31], v[48:51], v[12:15], v[28:31]\n"
      "v_dot2_f32_bf16 v5, v16, s58, v5\n"
      "v_dot2_f32_bf16 v6, v16, v16, v6\n"
      "ds_read_b64_tr_b16 v[48:49], v3 offset:6656\n"
      "ds_read_b64_tr_b16 v[50:51], v3 offset:6784\n"
      "s_waitcnt lgkmcnt(8)\n"
      "v_mfma_f32_16x16x32_bf16 v[32:35], v[52:55], v[12:15], v[32:35]\n"
      "v_dot2_f32_bf16 v5, v17, s58, v5\n"
      "v_dot2_f32_bf16 v6, v17, v17, v6\n"
      "ds_read_b64_tr_b16 v[52:53], v3 offset:11264\n"
      "ds_read_b64_tr_b16 v[54:55], v3 offset:11392\n"
      "s_waitcnt lgkmcnt(8)\n"
      "v_mfma_f32_16x16x32_bf16 v[36:39], v[56:59], v[12:15], v[36:39]\n"
      "v_dot2_f32_bf16 v5, v18, s58, v5\n"
      "v_dot2_f32_bf16 v6, v18, v18, v6\n"
      "ds_read_b64_tr_b16 v[56:57], v3 offset:15872\n"
      "ds_read_b64_tr_b16 v[58:59], v3 offset:16000\n"
      "s_waitcnt lgkmcnt(8)\n"
      "v_mfma_f32_16x16x32_bf16 v[40:43], v[60:63], v[12:15], v[40:43]\n"
      "v_dot2_f32_bf16 v5, v19, s58, v5\n"
      "v_dot2_f32_bf16 v6, v19, v19, v6\n"
      "ds_read_b64_tr_b16 v[60:61], v3 offset:3072\n"
      "ds_read_b64_tr_b16 v[62:63], v3 offset:3200\n"
      "ds_read_b64_tr_b16 v[12:13], v3 offset:7680\n"
      "ds_read_b64_tr_b16 v[14:15], v3 offset:7808\n"
      "s_waitcnt lgkmcnt(10)\n"
      "v_mfma_f32_16x16x32_bf16 v[28:31], v[8:11], v[16:19], v[28:31]\n"
      "v_dot2_f32_bf16 v5, v20, s58, v5\n"
      "v_dot2_f32_bf16 v6, v20, v20, v6\n"
      "ds_read_b64_tr_b16 v[8:9], v3 offset:12288\n"
      "ds_read_b64_tr_b16 v[10:11], v3 offset:12416\n"
      "s_waitcnt lgkmcnt(10)\n"
      "v_mfma_f32_16x16x32_bf16 v[32:35], v[48:51], v[16:19], v[32:35]\n"
      "v_dot2_f32_bf16 v5, v21, s58, v5\n"
      "v_dot2_f32_bf16 v6, v21, v21, v6\n"
      "ds_read_b64_tr_b16 v[48:49], v3 offset:16896\n"
      "ds_read_b64_tr_b16 v[50:51], v3 offset:17024\n"
      "s_waitcnt lgkmcnt(10)\n"
      "v_mfma_f32_16x16x32_bf16 v[36:39], v[52:55], v[16:19], v[36:39]\n"
      "v_dot2_f32_bf16 v5, v22, s58, v5\n"
      "v_dot2_f32_bf16 v6, v22, v22, v6\n"
      "ds_read_b64_tr_b16 v[52:53], v4 offset:4096\n"
      "ds_read_b64_tr_b16 v[54:55], v4 offset:4224\n"
      "s_waitcnt lgkmcnt(10)\n"
      "v_mfma_f32_16x16x32_bf16 v[40:43], v[56:59], v[16:19], v[40:43]\n"
      "v_dot2_f32_bf16 v5, v23, s58, v5\n"
      "v_dot2_f32_bf16 v6, v23, v23, v6\n"
      "ds_read_b64_tr_b16 v[56:57], v4 offset:8704\n"
      "ds_read_b64_tr_b16 v[58:59], v4 offset:8832\n"
      "ds_read_b64_tr_b16 v[16:17], v4 offset:13312\n"
      "ds_read_b64_tr_b16 v[18:19], v4 offset:13440\n"
      "s_waitcnt lgkmcnt(12)\n"
      "v_mfma_f32_16x16x32_bf16 v[28:31], v[60:63], v[20:23], v[28:31]\n"
      "v_dot2_f32_bf16 v5, v24, s58, v5\n"
      "v_dot2_f32_bf16 v6, v24, v24, v6\n"
      "ds_read_b64_tr_b16 v[60:61], v4 offset:17920\n"
      "ds_read_b64_tr_b16 v[62:63], v4 offset:18048\n"
      "s_waitcnt lgkmcnt(12)\n"
      "v_mfma_f32_16x16x32_bf16 v[32:35], v[12:15], v[20:23], v[32:35]\n"
      "v_dot2_f32_bf16 v5, v25, s58, v5\n"
      "v_dot2_f32_bf16 v6, v25, v25, v6\n"
      "s_waitcnt lgkmcnt(10)\n"
      "v_mfma_f32_16x16x32_bf16 v[36:39], v[8:11], v[20:23], v[36:39]\n"
      "v_dot2_f32_bf16 v5, v26, s58, v5\n"
      "v_dot2_f32_bf16 v6, v26, v26, v6\n"
      "s_waitcnt lgkmcnt(8)\n"
      "v_mfma_f32_16x16x32_bf16 v[40:43], v[48:51], v[20:23], v[40:43]\n"
      "v_dot2_f32_bf16 v5, v27, s58, v5\n"
      "v_dot2_f32_bf16 v6, v27, v27, v6\n"
      "s_waitcnt lgkmcnt(6)\n"
      "v_mfma_f32_16x16x32_bf16 v[28:31], v[52:55], v[24:27], v[28:31]\n"
      "s_nop 0\n"
      "v_permlane16_swap_b32 v5, v6\n"
      "v_add_f32 v5, v5, v6\n"
      "s_waitcnt lgkmcnt(4)\n"
      "v_mfma_f32_16x16x32_bf16 v[32:35], v[56:59], v[24:27], v[32:35]\n"
      "v_mov_b32 v6, v5\n"
      "s_waitcnt lgkmcnt(2)\n"
      "v_mfma_f32_16x16x32_bf16 v[36:39], v[16:19], v[24:27], v[36:39]\n"
      "v_permlane32_swap_b32 v5, v6\n"
      "v_add_f32 v5, v5, v6\n"
      "s_waitcnt lgkmcnt(0)\n"
      "v_mfma_f32_16x16x32_bf16 v[40:43], v[60:63], v[24:27], v[40:43]\n"
      "v_add_f32_dpp v5, v5, v5 row_ror:4 row_mask:0xf bank_mask:0xf\n"
      "s_nop 1\n"
      "v_add_f32_dpp v5, v5, v5 row_ror:8 row_mask:0xf bank_mask:0xf\n"
      "v_mov_b32 v6, v5\n"
      "s_nop 1\n"
      "v_permlane16_swap_b32 v5, v6\n"
      "v_mul_f32 v5, 0x3ae38e39, v5\n"
      "v_mul_f32 v6, 0x3ae38e39, v6\n"
      "v_fma_f32 v6, -v5, v5, v6\n"
      "v_max_f32 v6, 0, v6\n"
      "v_add_f32 v6, 0x3727c5ac, v6\n"
      "v_rsq_f32 v6, v6\n"
      "s_nop 0\n"
      "v_mul_f32 v5, v5, v6\n"
      "v_mul_f32 v44, v5, v44\n"
      "v_mul_f32 v45, v5, v45\n"
      "v_mul_f32 v46, v5, v46\n"
      "v_mul_f32 v47, v5, v47\n"
      "v_add_f32_dpp v28, v32, v28 row_shl:4 row_mask:0xf bank_mask:0xf\n"
      "v_add_f32_dpp v29, v33, v29 row_shl:4 row_mask:0xf bank_mask:0xf\n"
      "v_add_f32_dpp v30, v34, v30 row_shl:4 row_mask:0xf bank_mask:0xf\n"
      "v_add_f32_dpp v31, v35, v31 row_shl:4 row_mask:0xf bank_mask:0xf\n"
      "v_add_f32_dpp v36, v40, v36 row_shl:4 row_mask:0xf bank_mask:0xf\n"
      "v_add_f32_dpp v37, v41, v37 row_shl:4 row_mask:0xf bank_mask:0xf\n"
      "v_add_f32_dpp v38, v42, v38 row_shl:4 row_mask:0xf bank_mask:0xf\n"
      "v_add_f32_dpp v39, v43, v39 row_shl:4 row_mask:0xf bank_mask:0xf\n"
      "v_add_f32_dpp v28, v36, v28 row_shl:8 row_mask:0xf bank_mask:0xf\n"
      "v_add_f32_dpp v29, v37, v29 row_shl:8 row_mask:0xf bank_mask:0xf\n"
      "v_add_f32_dpp v30, v38, v30 row_shl:8 row_mask:0xf bank_mask:0xf\n"
      "v_add_f32_dpp v31, v39, v31 row_shl:8 row_mask:0xf bank_mask:0xf\n"
      "v_fma_f32 v28, v28, v6, -v44\n"
      "v_fma_f32 v29, v29, v6, -v45\n"
      "v_fma_f32 v30, v30, v6, -v46\n"
      "v_fma_f32 v31, v31, v6, -v47\n"
      "s_lshl_b32 s0, s45, 4\n"
      "s_add_u32 s0, s0, s46\n"
      "s_lshl_b32 s0, s0, 7\n"
      "s_add_u32 s0, s0, 8192\n"
      "v_cvt_pk_bf16_f32 v31, v30, v31\n"
      "v_cvt_pk_bf16_f32 v30, v28, v29\n"
      "v_min_u32 v31, -2, v31\n"
      "v_min_u32 v30, -2, v30\n"
      "s_nop 1\n"
      "v_permlane16_swap_b32 v30, v31\n"
      "s_nop 1\n"
      "v_permlane32_swap_b32 v28, v30\n"
      "v_permlane32_swap_b32 v29, v31\n"
      "s_mov_b32 exec_lo, 0\n"
      "s_mov_b32 exec_hi, 0x000f000f\n"
      "buffer_store_dwordx4 v[28:31], v7, s[16:19], s0 offen sc1\n"
      "s_mov_b64 exec, -1\n"
      ".Lw0_3_%=:\n"
      "v_and_b32 v63, 15, v0\n"
      "v_lshlrev_b32 v63, 8, v63\n"
      "v_and_or_b32 v63, v0, 48, v63\n"
      "v_lshlrev_b32 v62, 4, v0\n"
      "s_mul_i32 s1, s44, 4608\n"
      "s_add_u32 s1, s1, s32\n"
      "s_movk_i32 s0, 7282\n"
      "v_add_u32 v56, 0, v0\n"
      "v_mul_u32_u24 v57, s0, v56\n"
      "v_lshrrev_b32 v57, 16, v57\n"
      "v_mul_u32_u24 v57, 1008, v57\n"
      "v_lshl_add_u32 v56, v56, 4, v57\n"
      "s_add_u32 m0, s1, 0\n"
      "s_nop 0\n"
      "buffer_load_dwordx4 v56, s[24:27], 0 offen nt lds\n"
      "v_add_u32 v56, 64, v0\n"
      "v_mul_u32_u24 v57, s0, v56\n"
      "v_lshrrev_b32 v57, 16, v57\n"
      "v_mul_u32_u24 v57, 1008, v57\n"
      "v_lshl_add_u32 v56, v56, 4, v57\n"
      "s_add_u32 m0, s1, 1024\n"
      "s_nop 0\n"
      "buffer_load_dwordx4 v56, s[24:27], 0 offen nt lds\n"
      "v_add_u32 v56, 128, v0\n"
      "v_mul_u32_u24 v57, s0, v56\n"
      "v_lshrrev_b32 v57, 16, v57\n"
      "v_mul_u32_u24 v57, 1008, v57\n"
      "v_lshl_add_u32 v56, v56, 4, v57\n"
      "s_add_u32 m0, s1, 2048\n"
      "s_nop 0\n"
      "buffer_load_dwordx4 v56, s[24:27], 0 offen nt lds\n"
      "v_add_u32 v56, 192, v0\n"
      "v_mul_u32_u24 v57, s0, v56\n"
      "v_lshrrev_b32 v57, 16, v57\n"
      "v_mul_u32_u24 v57, 1008, v57\n"
      "v_lshl_add_u32 v56, v56, 4, v57\n"
      "s_add_u32 m0, s1, 3072\n"
      "s_nop 0\n"
      "buffer_load_dwordx4 v56, s[24:27], 0 offen nt lds\n"
      "v_add_u32 v56, 256, v0\n"
      "v_mul_u32_u24 v57, s0, v56\n"
      "v_lshrrev_b32 v57, 16, v57\n"
      "v_mul_u32_u24 v57, 1008, v57\n"
      "v_lshl_add_u32 v56, v56, 4, v57\n"
      "s_add_u32 m0, s1, 4096\n"
      "s_mov_b32 exec_hi, 0\n"
      "buffer_load_dwordx4 v56, s[24:27], 0 offen nt lds\n"
      "s_mov_b64 exec, -1\n"
      "s_add_u32 s0, s52, 5\n"
      "s_and_b32 s0, s0, 15\n"
      "s_lshl_b32 s0, s0, 12\n"
      "buffer_load_dwordx4 a[80:83], v63, s[8:11], s0 offen offset:0 nt\n"
      "buffer_load_dwordx4 a[84:87], v63, s[8:11], s0 offen offset:64 nt\n"
      "buffer_load_dwordx4 a[88:91], v63, s[8:11], s0 offen offset:128 nt\n"
      "buffer_load_dwordx4 a[92:95], v63, s[8:11], s0 offen offset:192 nt\n"
      "s_add_u32 s0, s52, 3\n"
      "s_and_b32 s0, s0, 15\n"
      "s_lshl_b32 s0, s0, 12\n"
      "buffer_load_dwordx4 v[76:79], v62, s[12:15], s0 offen offset:0 nt\n"
      "buffer_load_dwordx4 v[108:111], v62, s[12:15], s0 offen offset:1024 nt\n"
      "buffer_load_dwordx4 v[92:95], v62, s[12:15], s0 offen offset:2048 nt\n"
      "buffer_load_dwordx4 v[124:127], v62, s[12:15], s0 offen offset:3072 nt\n"
      "s_cmp_eq_u32 s44, 0\n"
      "s_cbranch_scc0 .Lw0_4_%=\n"
      "s_waitcnt vmcnt(13)\n"
      "v_mov_b32 v56, s50\n"
      "v_mov_b32 v57, s51\n"
      "s_lshl_b32 s0, s6, 3\n"
      "s_add_u32 s0, s0, 4096\n"
      "s_mov_b64 exec, 1\n"
      "buffer_store_dwordx2 v[56:57], off, s[16:19], s0 sc1\n"
      "s_mov_b64 exec, -1\n"
      ".Lw0_4_%=:\n"
      "s_add_u32 s0, s52, 6\n"
      "s_and_b32 s0, s0, 15\n"
      "s_lshl_b32 s0, s0, 12\n"
      "buffer_load_dwordx4 a[96:99], v63, s[8:11], s0 offen offset:0 nt\n"
      "buffer_load_dwordx4 a[100:103], v63, s[8:11], s0 offen offset:64 nt\n"
      "buffer_load_dwordx4 a[104:107], v63, s[8:11], s0 offen offset:128 nt\n"
      "buffer_load_dwordx4 a[108:111], v63, s[8:11], s0 offen offset:192 nt\n"
      "s_add_u32 s0, s52, 4\n"
      "s_and_b32 s0, s0, 15\n"
      "s_lshl_b32 s0, s0, 12\n"
      "buffer_load_dwordx4 v[128:131], v62, s[12:15], s0 offen offset:0 nt\n"
      "buffer_load_dwordx4 v[160:163], v62, s[12:15], s0 offen offset:1024 nt\n"
      "buffer_load_dwordx4 v[144:147], v62, s[12:15], s0 offen offset:2048 nt\n"
      "buffer_load_dwordx4 v[176:179], v62, s[12:15], s0 offen offset:3072 nt\n"
      "s_add_u32 s0, s52, 7\n"
      "s_and_b32 s0, s0, 15\n"
      "s_lshl_b32 s0, s0, 12\n"
      "buffer_load_dwordx4 a[112:115], v63, s[8:11], s0 offen offset:0 nt\n"
      "buffer_load_dwordx4 a[116:119], v63, s[8:11], s0 offen offset:64 nt\n"
      "buffer_load_dwordx4 a[120:123], v63, s[8:11], s0 offen offset:128 nt\n"
      "buffer_load_dwordx4 a[124:127], v63, s[8:11], s0 offen offset:192 nt\n"
      "s_add_u32 s0, s52, 5\n"
      "s_and_b32 s0, s0, 15\n"
      "s_lshl_b32 s0, s0, 12\n"
      "buffer_load_dwordx4 v[136:139], v62, s[12:15], s0 offen offset:0 nt\n"
      "buffer_load_dwordx4 v[168:171], v62, s[12:15], s0 offen offset:1024 nt\n"
      "buffer_load_dwordx4 v[152:155], v62, s[12:15], s0 offen offset:2048 nt\n"
      "buffer_load_dwordx4 v[184:187], v62, s[12:15], s0 offen offset:3072 nt\n"
      "s_waitcnt vmcnt(37)\n"
      "v_perm_b32 v56, v72, v64, s56\n"
      "v_perm_b32 v57, v72, v64, s57\n"
      "v_accvgpr_write_b32 a192, v56\n"
      "v_accvgpr_write_b32 a200, v57\n"
      "v_perm_b32 v58, v73, v65, s56\n"
      "v_perm_b32 v59, v73, v65, s57\n"
      "v_accvgpr_write_b32 a208, v58\n"
      "v_accvgpr_write_b32 a216, v59\n"
      "v_perm_b32 v56, v74, v66, s56\n"
      "v_perm_b32 v57, v74, v66, s57\n"
      "v_accvgpr_write_b32 a224, v56\n"
      "v_accvgpr_write_b32 a232, v57\n"
      "v_perm_b32 v58, v75, v67, s56\n"
      "v_perm_b32 v59, v75, v67, s57\n"
      "v_accvgpr_write_b32 a240, v58\n"
      "v_accvgpr_write_b32 a248, v59\n"
      "v_perm_b32 v56, v88, v80, s56\n"
      "v_perm_b32 v57, v88, v80, s57\n"
      "v_accvgpr_write_b32 a193, v56\n"
      "v_accvgpr_write_b32 a201, v57\n"
      "v_perm_b32 v58, v89, v81, s56\n"
      "v_perm_b32 v59, v89, v81, s57\n"
      "v_accvgpr_write_b32 a209, v58\n"
      "v_accvgpr_write_b32 a217, v59\n"
      "v_perm_b32 v56, v90, v82, s56\n"
      "v_perm_b32 v57, v90, v82, s57\n"
      "v_accvgpr_write_b32 a225, v56\n"
      "v_accvgpr_write_b32 a233, v57\n"
      "v_perm_b32 v58, v91, v83, s56\n"
      "v_perm_b32 v59, v91, v83, s57\n"
      "v_accvgpr_write_b32 a241, v58\n"
      "v_accvgpr_write_b32 a249, v59\n"
      "v_perm_b32 v56, v104, v96, s56\n"
      "v_perm_b32 v57, v104, v96, s57\n"
      "v_accvgpr_write_b32 a194, v56\n"
      "v_accvgpr_write_b32 a202, v57\n"
      "v_perm_b32 v58, v105, v97, s56\n"
      "v_perm_b32 v59, v105, v97, s57\n"
      "v_accvgpr_write_b32 a210, v58\n"
      "v_accvgpr_write_b32 a218, v59\n"
      "v_perm_b32 v56, v106, v98, s56\n"
      "v_perm_b32 v57, v106, v98, s57\n"
      "v_accvgpr_write_b32 a226, v56\n"
      "v_accvgpr_write_b32 a234, v57\n"
      "v_perm_b32 v58, v107, v99, s56\n"
      "v_perm_b32 v59, v107, v99, s57\n"
      "v_accvgpr_write_b32 a242, v58\n"
      "v_accvgpr_write_b32 a250, v59\n"
      "v_perm_b32 v56, v120, v112, s56\n"
      "v_perm_b32 v57, v120, v112, s57\n"
      "v_accvgpr_write_b32 a195, v56\n"
      "v_accvgpr_write_b32 a203, v57\n"
      "v_perm_b32 v58, v121, v113, s56\n"
      "v_perm_b32 v59, v121, v113, s57\n"
      "v_accvgpr_write_b32 a211, v58\n"
      "v_accvgpr_write_b32 a219, v59\n"
      "v_perm_b32 v56, v122, v114, s56\n"
      "v_perm_b32 v57, v122, v114, s57\n"
      "v_accvgpr_write_b32 a227, v56\n"
      "v_accvgpr_write_b32 a235, v57\n"
      "v_perm_b32 v58, v123, v115, s56\n"
      "v_perm_b32 v59, v123, v115, s57\n"
      "v_accvgpr_write_b32 a243, v58\n"
      "v_accvgpr_write_b32 a251, v59\n"
      "s_add_u32 s0, s52, 8\n"
      "s_and_b32 s0, s0, 15\n"
      "s_lshl_b32 s0, s0, 12\n"
      "buffer_load_dwordx4 a[128:131], v63, s[8:11], s0 offen offset:0 nt\n"
      "buffer_load_dwordx4 a[132:135], v63, s[8:11], s0 offen offset:64 nt\n"
      "buffer_load_dwordx4 a[136:139], v63, s[8:11], s0 offen offset:128 nt\n"
      "buffer_load_dwordx4 a[140:143], v63, s[8:11], s0 offen offset:192 nt\n"
      "s_add_u32 s0, s52, 6\n"
      "s_and_b32 s0, s0, 15\n"
      "s_lshl_b32 s0, s0, 12\n"
      "buffer_load_dwordx4 v[132:135], v62, s[12:15], s0 offen offset:0 nt\n"
      "buffer_load_dwordx4 v[164:167], v62, s[12:15], s0 offen offset:1024 nt\n"
      "buffer_load_dwordx4 v[148:151], v62, s[12:15], s0 offen offset:2048 nt\n"
      "buffer_load_dwordx4 v[180:183], v62, s[12:15], s0 offen offset:3072 nt\n"
      "s_add_u32 s0, s52, 9\n"
      "s_and_b32 s0, s0, 15\n"
      "s_lshl_b32 s0, s0, 12\n"
      "buffer_load_dwordx4 a[144:147], v63, s[8:11], s0 offen offset:0 nt\n"
      "buffer_load_dwordx4 a[148:151], v63, s[8:11], s0 offen offset:64 nt\n"
      "buffer_load_dwordx4 a[152:155], v63, s[8:11], s0 offen offset:128 nt\n"
      "buffer_load_dwordx4 a[156:159], v63, s[8:11], s0 offen offset:192 nt\n"
      "s_add_u32 s0, s52, 7\n"
      "s_and_b32 s0, s0, 15\n"
      "s_lshl_b32 s0, s0, 12\n"
      "buffer_load_dwordx4 v[140:143], v62, s[12:15], s0 offen offset:0 nt\n"
      "buffer_load_dwordx4 v[172:175], v62, s[12:15], s0 offen offset:1024 nt\n"
      "buffer_load_dwordx4 v[156:159], v62, s[12:15], s0 offen offset:2048 nt\n"
      "buffer_load_dwordx4 v[188:191], v62, s[12:15], s0 offen offset:3072 nt\n"
      "s_waitcnt vmcnt(32)\n"
      "v_perm_b32 v56, v76, v68, s56\n"
      "v_perm_b32 v57, v76, v68, s57\n"
      "v_accvgpr_write_b32 a196, v56\n"
      "v_accvgpr_write_b32 a204, v57\n"
      "v_perm_b32 v58, v77, v69, s56\n"
      "v_perm_b32 v59, v77, v69, s57\n"
      "v_accvgpr_write_b32 a212, v58\n"
      "v_accvgpr_write_b32 a220, v59\n"
      "v_perm_b32 v56, v78, v70, s56\n"
      "v_perm_b32 v57, v78, v70, s57\n"
      "v_accvgpr_write_b32 a228, v56\n"
      "v_accvgpr_write_b32 a236, v57\n"
      "v_perm_b32 v58, v79, v71, s56\n"
      "v_perm_b32 v59, v79, v71, s57\n"
      "v_accvgpr_write_b32 a244, v58\n"
      "v_accvgpr_write_b32 a252, v59\n"
      "v_perm_b32 v56, v92, v84, s56\n"
      "v_perm_b32 v57, v92, v84, s57\n"
      "v_accvgpr_write_b32 a197, v56\n"
      "v_accvgpr_write_b32 a205, v57\n"
      "v_perm_b32 v58, v93, v85, s56\n"
      "v_perm_b32 v59, v93, v85, s57\n"
      "v_accvgpr_write_b32 a213, v58\n"
      "v_accvgpr_write_b32 a221, v59\n"
      "v_perm_b32 v56, v94, v86, s56\n"
      "v_perm_b32 v57, v94, v86, s57\n"
      "v_accvgpr_write_b32 a229, v56\n"
      "v_accvgpr_write_b32 a237, v57\n"
      "v_perm_b32 v58, v95, v87, s56\n"
      "v_perm_b32 v59, v95, v87, s57\n"
      "v_accvgpr_write_b32 a245, v58\n"
      "v_accvgpr_write_b32 a253, v59\n"
      "v_perm_b32 v56, v108, v100, s56\n"
      "v_perm_b32 v57, v108, v100, s57\n"
      "v_accvgpr_write_b32 a198, v56\n"
      "v_accvgpr_write_b32 a206, v57\n"
      "v_perm_b32 v58, v109, v101, s56\n"
      "v_perm_b32 v59, v109, v101, s57\n"
      "v_accvgpr_write_b32 a214, v58\n"
      "v_accvgpr_write_b32 a222, v59\n"
      "v_perm_b32 v56, v110, v102, s56\n"
      "v_perm_b32 v57, v110, v102, s57\n"
      "v_accvgpr_write_b32 a230, v56\n"
      "v_accvgpr_write_b32 a238, v57\n"
      "v_perm_b32 v58, v111, v103, s56\n"
      "v_perm_b32 v59, v111, v103, s57\n"
      "v_accvgpr_write_b32 a246, v58\n"
      "v_accvgpr_write_b32 a254, v59\n"
      "v_perm_b32 v56, v124, v116, s56\n"
      "v_perm_b32 v57, v124, v116, s57\n"
      "v_accvgpr_write_b32 a199, v56\n"
      "v_accvgpr_write_b32 a207, v57\n"
      "v_perm_b32 v58, v125, v117, s56\n"
      "v_perm_b32 v59, v125, v117, s57\n"
      "v_accvgpr_write_b32 a215, v58\n"
      "v_accvgpr_write_b32 a223, v59\n"
      "v_perm_b32 v56, v126, v118, s56\n"
      "v_perm_b32 v57, v126, v118, s57\n"
      "v_accvgpr_write_b32 a231, v56\n"
      "v_accvgpr_write_b32 a239, v57\n"
      "v_perm_b32 v58, v127, v119, s56\n"
      "v_perm_b32 v59, v127, v119, s57\n"
      "v_accvgpr_write_b32 a247, v58\n"
      "v_accvgpr_write_b32 a255, v59\n"
      "s_add_u32 s0, s52, 10\n"
      "s_and_b32 s0, s0, 15\n"
      "s_lshl_b32 s0, s0, 12\n"
      "buffer_load_dwordx4 a[160:163], v63, s[8:11], s0 offen offset:0 nt\n"
      "buffer_load_dwordx4 a[164:167], v63, s[8:11], s0 offen offset:64 nt\n"
      "buffer_load_dwordx4 a[168:171], v63, s[8:11], s0 offen offset:128 nt\n"
      "buffer_load_dwordx4 a[172:175], v63, s[8:11], s0 offen offset:192 nt\n"
      "s_add_u32 s0, s52, 8\n"
      "s_and_b32 s0, s0, 15\n"
      "s_lshl_b32 s0, s0, 12\n"
      "buffer_load_dwordx4 v[192:195], v62, s[12:15], s0 offen offset:0 nt\n"
      "buffer_load_dwordx4 v[224:227], v62, s[12:15], s0 offen offset:1024 nt\n"
      "buffer_load_dwordx4 v[208:211], v62, s[12:15], s0 offen offset:2048 nt\n"
      "buffer_load_dwordx4 v[240:243], v62, s[12:15], s0 offen offset:3072 nt\n"
      "s_add_u32 s0, s52, 11\n"
      "s_and_b32 s0, s0, 15\n"
      "s_lshl_b32 s0, s0, 12\n"
      "buffer_load_dwordx4 a[176:179], v63, s[8:11], s0 offen offset:0 nt\n"
      "buffer_load_dwordx4 a[180:183], v63, s[8:11], s0 offen offset:64 nt\n"
      "buffer_load_dwordx4 a[184:187], v63, s[8:11], s0 offen offset:128 nt\n"
      "buffer_load_dwordx4 a[188:191], v63, s[8:11], s0 offen offset:192 nt\n"
      "s_add_u32 s0, s52, 9\n"
      "s_and_b32 s0, s0, 15\n"
      "s_lshl_b32 s0, s0, 12\n"
      "buffer_load_dwordx4 v[200:203], v62, s[12:15], s0 offen offset:0 nt\n"
      "buffer_load_dwordx4 v[232:235], v62, s[12:15], s0 offen offset:1024 nt\n"
      "buffer_load_dwordx4 v[216:219], v62, s[12:15], s0 offen offset:2048 nt\n"
      "buffer_load_dwordx4 v[248:251], v62, s[12:15], s0 offen offset:3072 nt\n"
      "s_waitcnt vmcnt(32)\n"
      "v_perm_b32 v56, v136, v128, s56\n"
      "v_perm_b32 v136, v136, v128, s57\n"
      "v_mov_b32 v128, v56\n"
      "v_perm_b32 v56, v137, v129, s56\n"
      "v_perm_b32 v57, v137, v129, s57\n"
      "v_perm_b32 v129, v152, v144, s56\n"
      "v_perm_b32 v137, v152, v144, s57\n"
      "v_mov_b32 v144, v56\n"
      "v_mov_b32 v152, v57\n"
      "v_perm_b32 v56, v138, v130, s56\n"
      "v_perm_b32 v57, v138, v130, s57\n"
      "v_perm_b32 v130, v168, v160, s56\n"
      "v_perm_b32 v138, v168, v160, s57\n"
      "v_mov_b32 v160, v56\n"
      "v_mov_b32 v168, v57\n"
      "v_perm_b32 v56, v139, v131, s56\n"
      "v_perm_b32 v57, v139, v131, s57\n"
      "v_perm_b32 v131, v184, v176, s56\n"
      "v_perm_b32 v139, v184, v176, s57\n"
      "v_mov_b32 v176, v56\n"
      "v_mov_b32 v184, v57\n"
      "v_perm_b32 v56, v153, v145, s56\n"
      "v_perm_b32 v153, v153, v145, s57\n"
      "v_mov_b32 v145, v56\n"
      "v_perm_b32 v56, v154, v146, s56\n"
      "v_perm_b32 v57, v154, v146, s57\n"
      "v_perm_b32 v146, v169, v161, s56\n"
      "v_perm_b32 v154, v169, v161, s57\n"
      "v_mov_b32 v161, v56\n"
      "v_mov_b32 v169, v57\n"
      "v_perm_b32 v56, v155, v147, s56\n"
      "v_perm_b32 v57, v155, v147, s57\n"
      "v_perm_b32 v147, v185, v177, s56\n"
      "v_perm_b32 v155, v185, v177, s57\n"
      "v_mov_b32 v177, v56\n"
      "v_mov_b32 v185, v57\n"
      "v_perm_b32 v56, v170, v162, s56\n"
      "v_perm_b32 v170, v170, v162, s57\n"
      "v_mov_b32 v162, v56\n"
      "v_perm_b32 v56, v171, v163, s56\n"
      "v_perm_b32 v57, v171, v163, s57\n"
      "v_perm_b32 v163, v186, v178, s56\n"
      "v_perm_b32 v171, v186, v178, s57\n"
      "v_mov_b32 v178, v56\n"
      "v_mov_b32 v186, v57\n"
      "v_perm_b32 v56, v187, v179, s56\n"
      "v_perm_b32 v187, v187, v179, s57\n"
      "v_mov_b32 v179, v56\n"
      "s_add_u32 s0, s52, 12\n"
      "s_and_b32 s0, s0, 15\n"
      "s_lshl_b32 s0, s0, 12\n"
      "s_add_u32 m0, s59, 0\n"
      "s_nop 0\n"
      "buffer_load_dwordx4 v63, s[8:11], s0 offen offset:0 nt lds\n"
      "s_add_u32 m0, s59, 960\n"
      "s_nop 0\n"
      "buffer_load_dwordx4 v63, s[8:11], s0 offen offset:64 nt lds\n"
      "s_add_u32 m0, s59, 1920\n"
      "s_nop 0\n"
      "buffer_load_dwordx4 v63, s[8:11], s0 offen offset:128 nt lds\n"
      "s_add_u32 m0, s59, 2880\n"
      "s_nop 0\n"
      "buffer_load_dwordx4 v63, s[8:11], s0 offen offset:192 nt lds\n"
      "s_add_u32 s0, s52, 10\n"
      "s_and_b32 s0, s0, 15\n"
      "s_lshl_b32 s0, s0, 12\n"
      "buffer_load_dwordx4 v[196:199], v62, s[12:15], s0 offen offset:0 nt\n"
      "buffer_load_dwordx4 v[228:231], v62, s[12:15], s0 offen offset:1024 nt\n"
      "buffer_load_dwordx4 v[212:215], v62, s[12:15], s0 offen offset:2048 nt\n"
      "buffer_load_dwordx4 v[244:247], v62, s[12:15], s0 offen offset:3072 nt\n"
      "s_add_u32 s0, s52, 13\n"
      "s_and_b32 s0, s0, 15\n"
      "s_lshl_b32 s0, s0, 12\n"
      "s_add_u32 m0, s59, 4096\n"
      "s_nop 0\n"
      "buffer_load_dwordx4 v63, s[8:11], s0 offen offset:0 nt lds\n"
      "s_add_u32 m0, s59, 5056\n"
      "s_nop 0\n"
      "buffer_load_dwordx4 v63, s[8:11], s0 offen offset:64 nt lds\n"
      "s_add_u32 m0, s59, 6016\n"
      "s_nop 0\n"
      "buffer_load_dwordx4 v63, s[8:11], s0 offen offset:128 nt lds\n"
      "s_add_u32 m0, s59, 6976\n"
      "s_nop 0\n"
      "buffer_load_dwordx4 v63, s[8:11], s0 offen offset:192 nt lds\n"
      "s_add_u32 s0, s52, 11\n"
      "s_and_b32 s0, s0, 15\n"
      "s_lshl_b32 s0, s0, 12\n"
      "buffer_load_dwordx4 v[204:207], v62, s[12:15], s0 offen offset:0 nt\n"
      "buffer_load_dwordx4 v[236:239], v62, s[12:15], s0 offen offset:1024 nt\n"
      "buffer_load_dwordx4 v[220:223], v62, s[12:15], s0 offen offset:2048 nt\n"
      "buffer_load_dwordx4 v[252:255], v62, s[12:15], s0 offen offset:3072 nt\n"
      "v_lshlrev_b32 v60, 5, v0\n"
      "s_movk_i32 s61, 4096\n"
      "buffer_load_dwordx4 v[48:51], v60, s[16:19], s61 offen sc1\n"
      "buffer_load_dwordx4 v[52:55], v60, s[16:19], s61 offen offset:16 sc1\n"
      "s_waitcnt vmcnt(34)\n"
      "v_perm_b32 v56, v140, v132, s56\n"
      "v_perm_b32 v140, v140, v132, s57\n"
      "v_mov_b32 v132, v56\n"
      "v_perm_b32 v56, v141, v133, s56\n"
      "v_perm_b32 v57, v141, v133, s57\n"
      "v_perm_b32 v133, v156, v148, s56\n"
      "v_perm_b32 v141, v156, v148, s57\n"
      "v_mov_b32 v148, v56\n"
      "v_mov_b32 v156, v57\n"
      "v_perm_b32 v56, v142, v134, s56\n"
      "v_perm_b32 v57, v142, v134, s57\n"
      "v_perm_b32 v134, v172, v164, s56\n"
      "v_perm_b32 v142, v172, v164, s57\n"
      "v_mov_b32 v164, v56\n"
      "v_mov_b32 v172, v57\n"
      "v_perm_b32 v56, v143, v135, s56\n"
      "v_perm_b32 v57, v143, v135, s57\n"
      "v_perm_b32 v135, v188, v180, s56\n"
      "v_perm_b32 v143, v188, v180, s57\n"
      "v_mov_b32 v180, v56\n"
      "v_mov_b32 v188, v57\n"
      "v_perm_b32 v56, v157, v149, s56\n"
      "v_perm_b32 v157, v157, v149, s57\n"
      "v_mov_b32 v149, v56\n"
      "v_perm_b32 v56, v158, v150, s56\n"
      "v_perm_b32 v57, v158, v150, s57\n"
      "v_perm_b32 v150, v173, v165, s56\n"
      "v_perm_b32 v158, v173, v165, s57\n"
      "v_mov_b32 v165, v56\n"
      "v_mov_b32 v173, v57\n"
      "v_perm_b32 v56, v159, v151, s56\n"
      "v_perm_b32 v57, v159, v151, s57\n"
      "v_perm_b32 v151, v189, v181, s56\n"
      "v_perm_b32 v159, v189, v181, s57\n"
      "v_mov_b32 v181, v56\n"
      "v_mov_b32 v189, v57\n"
      "v_perm_b32 v56, v174, v166, s56\n"
      "v_perm_b32 v174, v174, v166, s57\n"
      "v_mov_b32 v166, v56\n"
      "v_perm_b32 v56, v175, v167, s56\n"
      "v_perm_b32 v57, v175, v167, s57\n"
      "v_perm_b32 v167, v190, v182, s56\n"
      "v_perm_b32 v175, v190, v182, s57\n"
      "v_mov_b32 v182, v56\n"
      "v_mov_b32 v190, v57\n"
      "v_perm_b32 v56, v191, v183, s56\n"
      "v_perm_b32 v191, v191, v183, s57\n"
      "v_mov_b32 v183, v56\n"
      "s_add_u32 s0, s52, 14\n"
      "s_and_b32 s0, s0, 15\n"
      "s_lshl_b32 s0, s0, 12\n"
      "s_add_u32 m0, s59, 8192\n"
      "s_nop 0\n"
      "buffer_load_dwordx4 v63, s[8:11], s0 offen offset:0 nt lds\n"
      "s_add_u32 m0, s59, 9152\n"
      "s_nop 0\n"
      "buffer_load_dwordx4 v63, s[8:11], s0 offen offset:64 nt lds\n"
      "s_add_u32 m0, s59, 10112\n"
      "s_nop 0\n"
      "buffer_load_dwordx4 v63, s[8:11], s0 offen offset:128 nt lds\n"
      "s_add_u32 m0, s59, 11072\n"
      "s_nop 0\n"
      "buffer_load_dwordx4 v63, s[8:11], s0 offen offset:192 nt lds\n"
      "s_add_u32 s0, s52, 12\n"
      "s_and_b32 s0, s0, 15\n"
      "s_lshl_b32 s0, s0, 12\n"
      "buffer_load_dwordx4 v[64:67], v62, s[12:15], s0 offen offset:0 nt\n"
      "buffer_load_dwordx4 v[96:99], v62, s[12:15], s0 offen offset:1024 nt\n"
      "buffer_load_dwordx4 v[80:83], v62, s[12:15], s0 offen offset:2048 nt\n"
      "buffer_load_dwordx4 v[112:115], v62, s[12:15], s0 offen offset:3072 nt\n"
      "s_add_u32 s0, s52, 15\n"
      "s_and_b32 s0, s0, 15\n"
      "s_lshl_b32 s0, s0, 12\n"
      "s_add_u32 m0, s59, 12288\n"
      "s_nop 0\n"
      "buffer_load_dwordx4 v63, s[8:11], s0 offen offset:0 nt lds\n"
      "s_add_u32 m0, s59, 13248\n"
      "s_nop 0\n"
      "buffer_load_dwordx4 v63, s[8:11], s0 offen offset:64 nt lds\n"
      "s_add_u32 m0, s59, 14208\n"
      "s_nop 0\n"
      "buffer_load_dwordx4 v63, s[8:11], s0 offen offset:128 nt lds\n"
      "s_add_u32 m0, s59, 15168\n"
      "s_nop 0\n"
      "buffer_load_dwordx4 v63, s[8:11], s0 offen offset:192 nt lds\n"
      "s_add_u32 s0, s52, 13\n"
      "s_and_b32 s0, s0, 15\n"
      "s_lshl_b32 s0, s0, 12\n"
      "buffer_load_dwordx4 v[72:75], v62, s[12:15], s0 offen offset:0 nt\n"
      "buffer_load_dwordx4 v[104:107], v62, s[12:15], s0 offen offset:1024 nt\n"
      "buffer_load_dwordx4 v[88:91], v62, s[12:15], s0 offen offset:2048 nt\n"
      "buffer_load_dwordx4 v[120:123], v62, s[12:15], s0 offen offset:3072 nt\n"
      "v_and_b32 v5, 1, v0\n"
      "v_mov_b32 v1, 0x4444\n"
      "v_mul_u32_u24 v6, 0xaaaa, v5\n"
      "v_add_u32 v1, v1, v6\n"
      "v_lshl_add_u32 v2, v0, 4, s59\n"
      "v_bfe_u32 v6, v0, 1, 1\n"
      "v_lshrrev_b32 v7, 4, v0\n"
      "s_lshl_b32 s0, s44, 10\n"
      "s_add_u32 s0, s0, 157696\n"
      "v_lshlrev_b32 v4, 6, v5\n"
      "v_lshl_add_u32 v4, v6, 7, v4\n"
      "v_lshl_add_u32 v4, v7, 4, v4\n"
      "v_add_u32 v4, s0, v4\n"
      "v_lshlrev_b32 v3, 3, v5\n"
      "v_lshl_add_u32 v3, v6, 4, v3\n"
      "v_lshl_add_u32 v3, v7, 8, v3\n"
      "v_and_or_b32 v3, v0, 4, v3\n"
      "v_bfe_u32 v8, v0, 3, 1\n"
      "v_lshl_add_u32 v3, v8, 6, v3\n"
      "v_add_u32 v3, s0, v3\n"
      "v_lshl_add_u32 v6, v6, 3, v7\n"
      "v_lshlrev_b32 v6, 7, v6\n"
      "v_lshl_add_u32 v7, v5, 4, v6\n"
      "v_mov_b32 v9, 0x40000000\n"
      "v_cmp_eq_u32 vcc, 0, v8\n"
      "s_nop 1\n"
      "v_cndmask_b32 v5, v9, v7, vcc\n"
      "v_cndmask_b32 v6, v7, v9, vcc\n"
      "v_mov_b32 v7, 0x3e0293ee\n"
      "v_mov_b64 v[24:25], 0\n"
      "v_mov_b64 v[26:27], 0\n"
      "v_mov_b64 v[28:29], 0\n"
      "v_mov_b64 v[30:31], 0\n"
      "v_mov_b64 v[32:33], 0\n"
      "v_mov_b64 v[34:35], 0\n"
      "v_mov_b64 v[36:37], 0\n"
      "v_mov_b64 v[38:39], 0\n"
      "v_mov_b64 v[40:41], 0\n"
      "v_mov_b64 v[42:43], 0\n"
      "v_mov_b64 v[44:45], 0\n"
      "v_mov_b64 v[46:47], 0\n"
      "s_waitcnt vmcnt(16)\n"
      "v_cmp_ne_u64 vcc, s[50:51], v[48:49]\n"
      "v_cmp_ne_u64 s[0:1], s[50:51], v[50:51]\n"
      "v_cmp_ne_u64 s[2:3], s[50:51], v[52:53]\n"
      "s_or_b64 vcc, vcc, s[0:1]\n"
      "v_cmp_ne_u64 s[0:1], s[50:51], v[54:55]\n"
      "s_or_b64 vcc, vcc, s[2:3]\n"
      "s_or_b64 vcc, vcc, s[0:1]\n"
      "s_cbranch_scc0 .Lok_5_%=\n"
      "s_mov_b32 s54, 262144\n"
      ".Lsp_6_%=:\n"
      "buffer_load_dwordx4 v[48:51], v60, s[16:19], s61 offen sc1\n"
      "buffer_load_dwordx4 v[52:55], v60, s[16:19], s61 offen offset:16 sc1\n"
      "s_waitcnt vmcnt(0)\n"
      "v_cmp_ne_u64 vcc, s[50:51], v[48:49]\n"
      "v_cmp_ne_u64 s[0:1], s[50:51], v[50:51]\n"
      "v_cmp_ne_u64 s[2:3], s[50:51], v[52:53]\n"
      "s_or_b64 vcc, vcc, s[0:1]\n"
      "v_cmp_ne_u64 s[0:1], s[50:51], v[54:55]\n"
      "s_or_b64 vcc, vcc, s[2:3]\n"
      "s_or_b64 vcc, vcc, s[0:1]\n"
      "s_cbranch_scc0 .Lok_5_%=\n"
      "s_sub_u32 s54, s54, 1\n"
      "s_nop 0\n"
      "s_cbranch_scc0 .Lsp_6_%=\n"
      ".Lok_5_%=:\n"
      "s_lshl_b32 s1, s45, 11\n"
      "s_lshl_b32 s0, s60, 5\n"
      "s_add_u32 s1, s1, s0\n"
      "s_add_u32 s1, s1, 8192\n"
      "buffer_load_dwordx4 v[8:11], v5, s[16:19], s1 offen offset:0 sc1\n"
      "buffer_load_dwordx4 v[12:15], v5, s[16:19], s1 offen offset:512 sc1\n"
      "buffer_load_dwordx4 v[16:19], v6, s[16:19], s1 offen offset:0 sc1\n"
      "buffer_load_dwordx4 v[20:23], v6, s[16:19], s1 offen offset:512 sc1\n"
      "v_perm_b32 v56, v200, v192, s56\n"
      "v_perm_b32 v200, v200, v192, s57\n"
      "v_mov_b32 v192, v56\n"
      "v_perm_b32 v56, v201, v193, s56\n"
      "v_perm_b32 v57, v201, v193, s57\n"
      "v_perm_b32 v193, v216, v208, s56\n"
      "v_perm_b32 v201, v216, v208, s57\n"
      "v_mov_b32 v208, v56\n"
      "v_mov_b32 v216, v57\n"
      "v_perm_b32 v56, v202, v194, s56\n"
      "v_perm_b32 v57, v202, v194, s57\n"
      "v_perm_b32 v194, v232, v224, s56\n"
      "v_perm_b32 v202, v232, v224, s57\n"
      "v_mov_b32 v224, v56\n"
      "v_mov_b32 v232, v57\n"
      "v_perm_b32 v56, v203, v195, s56\n"
      "v_perm_b32 v57, v203, v195, s57\n"
      "v_perm_b32 v195, v248, v240, s56\n"
      "v_perm_b32 v203, v248, v240, s57\n"
      "v_mov_b32 v240, v56\n"
      "v_mov_b32 v248, v57\n"
      "v_perm_b32 v56, v217, v209, s56\n"
      "v_perm_b32 v217, v217, v209, s57\n"
      "v_mov_b32 v209, v56\n"
      "v_perm_b32 v56, v218, v210, s56\n"
      "v_perm_b32 v57, v218, v210, s57\n"
      "v_perm_b32 v210, v233, v225, s56\n"
      "v_perm_b32 v218, v233, v225, s57\n"
      "v_mov_b32 v225, v56\n"
      "v_mov_b32 v233, v57\n"
      "v_perm_b32 v56, v219, v211, s56\n"
      "v_perm_b32 v57, v219, v211, s57\n"
      "v_perm_b32 v211, v249, v241, s56\n"
      "v_perm_b32 v219, v249, v241, s57\n"
      "v_mov_b32 v241, v56\n"
      "v_mov_b32 v249, v57\n"
      "v_perm_b32 v56, v234, v226, s56\n"
      "v_perm_b32 v234, v234, v226, s57\n"
      "v_mov_b32 v226, v56\n"
      "v_perm_b32 v56, v235, v227, s56\n"
      "v_perm_b32 v57, v235, v227, s57\n"
      "v_perm_b32 v227, v250, v242, s56\n"
      "v_perm_b32 v235, v250, v242, s57\n"
      "v_mov_b32 v242, v56\n"
      "v_mov_b32 v250, v57\n"
      "v_perm_b32 v56, v251, v243, s56\n"
      "v_perm_b32 v251, v251, v243, s57\n"
      "v_mov_b32 v243, v56\n"
      "s_add_u32 s0, s52, 14\n"
      "s_and_b32 s0, s0, 15\n"
      "s_lshl_b32 s0, s0, 12\n"
      "buffer_load_dwordx4 v[68:71], v62, s[12:15], s0 offen offset:0 nt\n"
      "buffer_load_dwordx4 v[100:103], v62, s[12:15], s0 offen offset:1024 nt\n"
      "buffer_load_dwordx4 v[84:87], v62, s[12:15], s0 offen offset:2048 nt\n"
      "buffer_load_dwordx4 v[116:119], v62, s[12:15], s0 offen offset:3072 nt\n"
      "s_add_u32 s0, s52, 15\n"
      "s_and_b32 s0, s0, 15\n"
      "s_lshl_b32 s0, s0, 12\n"
      "buffer_load_dwordx4 v[76:79], v62, s[12:15], s0 offen offset:0 nt\n"
      "buffer_load_dwordx4 v[108:111], v62, s[12:15], s0 offen offset:1024 nt\n"
      "buffer_load_dwordx4 v[92:95], v62, s[12:15], s0 offen offset:2048 nt\n"
      "buffer_load_dwordx4 v[124:127], v62, s[12:15], s0 offen offset:3072 nt\n"
      "v_perm_b32 v56, v204, v196, s56\n"
      "v_perm_b32 v204, v204, v196, s57\n"
      "v_mov_b32 v196, v56\n"
      "v_perm_b32 v56, v205, v197, s56\n"
      "v_perm_b32 v57, v205, v197, s57\n"
      "v_perm_b32 v197, v220, v212, s56\n"
      "v_perm_b32 v205, v220, v212, s57\n"
      "v_mov_b32 v212, v56\n"
      "v_mov_b32 v220, v57\n"
      "v_perm_b32 v56, v206, v198, s56\n"
      "v_perm_b32 v57, v206, v198, s57\n"
      "v_perm_b32 v198, v236, v228, s56\n"
      "v_perm_b32 v206, v236, v228, s57\n"
      "v_mov_b32 v228, v56\n"
      "v_mov_b32 v236, v57\n"
      "v_perm_b32 v56, v207, v199, s56\n"
      "v_perm_b32 v57, v207, v199, s57\n"
      "v_perm_b32 v199, v252, v244, s56\n"
      "v_perm_b32 v207, v252, v244, s57\n"
      "v_mov_b32 v244, v56\n"
      "v_mov_b32 v252, v57\n"
      "v_perm_b32 v56, v221, v213, s56\n"
      "v_perm_b32 v221, v221, v213, s57\n"
      "v_mov_b32 v213, v56\n"
      "v_perm_b32 v56, v222, v214, s56\n"
      "v_perm_b32 v57, v222, v214, s57\n"
      "v_perm_b32 v214, v237, v229, s56\n"
      "v_perm_b32 v222, v237, v229, s57\n"
      "v_mov_b32 v229, v56\n"
      "v_mov_b32 v237, v57\n"
      "v_perm_b32 v56, v223, v215, s56\n"
      "v_perm_b32 v57, v223, v215, s57\n"
      "v_perm_b32 v215, v253, v245, s56\n"
      "v_perm_b32 v223, v253, v245, s57\n"
      "v_mov_b32 v245, v56\n"
      "v_mov_b32 v253, v57\n"
      "v_perm_b32 v56, v238, v230, s56\n"
      "v_perm_b32 v238, v238, v230, s57\n"
      "v_mov_b32 v230, v56\n"
      "v_perm_b32 v56, v239, v231, s56\n"
      "v_perm_b32 v57, v239, v231, s57\n"
      "v_perm_b32 v231, v254, v246, s56\n"
      "v_perm_b32 v239, v254, v246, s57\n"
      "v_mov_b32 v246, v56\n"
      "v_mov_b32 v254, v57\n"
      "v_perm_b32 v56, v255, v247, s56\n"
      "v_perm_b32 v255, v255, v247, s57\n"
      "v_mov_b32 v247, v56\n"
      "s_waitcnt vmcnt(8)\n"
      "ds_read_b128 v[48:51], v2 offset:0\n"
      "ds_read_b128 v[52:55], v2 offset:1024\n"
      "ds_read_b128 v[56:59], v2 offset:2048\n"
      "ds_read_b128 v[60:63], v2 offset:3072\n"
      ".Latt_7_%=:\n"
      "v_smfmac_f32_16x16x64_bf16 v[24:27], v[8:11], a[0:7], v1\n"
      "v_smfmac_f32_16x16x64_bf16 v[28:31], v[8:11], a[64:71], v1\n"
      "v_smfmac_f32_16x16x64_bf16 v[24:27], v[12:15], a[8:15], v1\n"
      "v_smfmac_f32_16x16x64_bf16 v[28:31], v[12:15], a[72:79], v1\n"
      "v_smfmac_f32_16x16x64_bf16 v[24:27], v[16:19], a[128:135], v1\n"
      "s_waitcnt lgkmcnt(2)\n"
      "v_smfmac_f32_16x16x64_bf16 v[28:31], v[16:19], v[48:55], v1\n"
      "ds_read_b128 v[48:51], v2 offset:4096\n"
      "ds_read_b128 v[52:55], v2 offset:5120\n"
      "v_smfmac_f32_16x16x64_bf16 v[24:27], v[20:23], a[136:143], v1\n"
      "s_waitcnt lgkmcnt(2)\n"
      "v_smfmac_f32_16x16x64_bf16 v[28:31], v[20:23], v[56:63], v1\n"
      "ds_read_b128 v[56:59], v2 offset:6144\n"
      "ds_read_b128 v[60:63], v2 offset:7168\n"
      "v_smfmac_f32_16x16x64_bf16 v[32:35], v[8:11], a[16:23], v1\n"
      "v_smfmac_f32_16x16x64_bf16 v[36:39], v[8:11], a[80:87], v1\n"
      "v_smfmac_f32_16x16x64_bf16 v[32:35], v[12:15], a[24:31], v1\n"
      "s_nop 0\n"
      "v_add_f32_dpp v24, v24, v25 quad_perm:[0,1,2,3] row_mask:0x5 bank_mask:0xf\n"
      "v_smfmac_f32_16x16x64_bf16 v[36:39], v[12:15], a[88:95], v1\n"
      "v_add_f32_dpp v26, v26, v27 quad_perm:[0,1,2,3] row_mask:0x5 bank_mask:0xf\n"
      "v_smfmac_f32_16x16x64_bf16 v[32:35], v[16:19], a[144:151], v1\n"
      "v_add_f32_dpp v24, v28, v29 quad_perm:[0,1,2,3] row_mask:0xa bank_mask:0xf\n"
      "v_add_f32_dpp v26, v30, v31 quad_perm:[0,1,2,3] row_mask:0xa bank_mask:0xf\n"
      "s_waitcnt lgkmcnt(2)\n"
      "v_smfmac_f32_16x16x64_bf16 v[36:39], v[16:19], v[48:55], v1\n"
      "ds_read_b128 v[48:51], v2 offset:8192\n"
      "ds_read_b128 v[52:55], v2 offset:9216\n"
      "v_smfmac_f32_16x16x64_bf16 v[32:35], v[20:23], a[152:159], v1\n"
      "v_mov_b64 v[28:29], 0\n"
      "s_waitcnt lgkmcnt(2)\n"
      "v_smfmac_f32_16x16x64_bf16 v[36:39], v[20:23], v[56:63], v1\n"
      "ds_read_b128 v[56:59], v2 offset:10240\n"
      "ds_read_b128 v[60:63], v2 offset:11264\n"
      "v_mov_b64 v[30:31], 0\n"
      "v_smfmac_f32_16x16x64_bf16 v[40:43], v[8:11], a[32:39], v1\n"
      "v_smfmac_f32_16x16x64_bf16 v[44:47], v[8:11], a[96:103], v1\n"
      "v_smfmac_f32_16x16x64_bf16 v[40:43], v[12:15], a[40:47], v1\n"
      "v_add_f32_dpp v32, v32, v33 quad_perm:[0,1,2,3] row_mask:0x5 bank_mask:0xf\n"
      "v_smfmac_f32_16x16x64_bf16 v[44:47], v[12:15], a[104:111], v1\n"
      "v_add_f32_dpp v34, v34, v35 quad_perm:[0,1,2,3] row_mask:0x5 bank_mask:0xf\n"
      "v_smfmac_f32_16x16x64_bf16 v[40:43], v[16:19], a[160:167], v1\n"
      "v_add_f32_dpp v32, v36, v37 quad_perm:[0,1,2,3] row_mask:0xa bank_mask:0xf\n"
      "v_add_f32_dpp v34, v38, v39 quad_perm:[0,1,2,3] row_mask:0xa bank_mask:0xf\n"
      "s_waitcnt lgkmcnt(2)\n"
      "v_smfmac_f32_16x16x64_bf16 v[44:47], v[16:19], v[48:55], v1\n"
      "ds_read_b128 v[48:51], v2 offset:12288\n"
      "ds_read_b128 v[52:55], v2 offset:13312\n"
      "v_smfmac_f32_16x16x64_bf16 v[40:43], v[20:23], a[168:175], v1\n"
      "v_mov_b64 v[36:37], 0\n"
      "s_waitcnt lgkmcnt(2)\n"
      "v_smfmac_f32_16x16x64_bf16 v[44:47], v[20:23], v[56:63], v1\n"
      "ds_read_b128 v[56:59], v2 offset:14336\n"
      "ds_read_b128 v[60:63], v2 offset:15360\n"
      "v_mov_b64 v[38:39], 0\n"
      "v_smfmac_f32_16x16x64_bf16 v[28:31], v[8:11], a[48:55], v1\n"
      "s_nop 0\n"
      "v_smfmac_f32_16x16x64_bf16 v[36:39], v[8:11], a[112:119], v1\n"
      "v_smfmac_f32_16x16x64_bf16 v[28:31], v[12:15], a[56:63], v1\n"
      "v_add_f32_dpp v40, v40, v41 quad_perm:[0,1,2,3] row_mask:0x5 bank_mask:0xf\n"
      "v_smfmac_f32_16x16x64_bf16 v[36:39], v[12:15], a[120:127], v1\n"
      "v_add_f32_dpp v42, v42, v43 quad_perm:[0,1,2,3] row_mask:0x5 bank_mask:0xf\n"
      "v_smfmac_f32_16x16x64_bf16 v[28:31], v[16:19], a[176:183], v1\n"
      "v_add_f32_dpp v40, v44, v45 quad_perm:[0,1,2,3] row_mask:0xa bank_mask:0xf\n"
      "v_add_f32_dpp v42, v46, v47 quad_perm:[0,1,2,3] row_mask:0xa bank_mask:0xf\n"
      "s_waitcnt lgkmcnt(2)\n"
      "v_smfmac_f32_16x16x64_bf16 v[36:39], v[16:19], v[48:55], v1\n"
      "v_smfmac_f32_16x16x64_bf16 v[28:31], v[20:23], a[184:191], v1\n"
      "s_waitcnt lgkmcnt(0)\n"
      "v_smfmac_f32_16x16x64_bf16 v[36:39], v[20:23], v[56:63], v1\n"
      "v_max3_f32 v60, v24, v32, v40\n"
      "v_max3_f32 v61, v26, v34, v42\n"
      "v_and_b32 v2, 15, v0\n"
      "v_lshrrev_b32 v5, 4, v0\n"
      "v_lshlrev_b32 v2, 5, v2\n"
      "v_lshl_add_u32 v2, v5, 3, v2\n"
      "s_lshl_b32 s0, s44, 10\n"
      "s_add_u32 s0, s0, 157696\n"
      "v_add_u32 v2, s0, v2\n"
      "v_add_f32_dpp v28, v28, v29 quad_perm:[0,1,2,3] row_mask:0x5 bank_mask:0xf\n"
      "v_add_f32_dpp v30, v30, v31 quad_perm:[0,1,2,3] row_mask:0x5 bank_mask:0xf\n"
      "s_nop 0\n"
      "v_add_f32_dpp v28, v36, v37 quad_perm:[0,1,2,3] row_mask:0xa bank_mask:0xf\n"
      "v_add_f32_dpp v30, v38, v39 quad_perm:[0,1,2,3] row_mask:0xa bank_mask:0xf\n"
      "v_max_f32 v60, v60, v28\n"
      "v_max_f32 v61, v61, v30\n"
      "s_nop 1\n"
      "v_permlane16_swap_b32 v60, v61\n"
      "v_max_f32 v60, v60, v61\n"
      "v_mov_b32 v61, v60\n"
      "s_nop 1\n"
      "v_permlane32_swap_b32 v60, v61\n"
      "v_max_f32 v60, v60, v61\n"
      "s_nop 1\n"
      "v_max_f32_dpp v60, v60, v60 row_ror:8 row_mask:0xf bank_mask:0xf\n"
      "s_nop 1\n"
      "v_max_f32_dpp v60, v60, v60 row_ror:4 row_mask:0xf bank_mask:0xf\n"
      "s_nop 1\n"
      "v_max_f32_dpp v60, v60, v60 row_ror:2 row_mask:0xf bank_mask:0xf\n"
      "s_nop 1\n"
      "v_max_f32_dpp v60, v60, v60 row_ror:1 row_mask:0xf bank_mask:0xf\n"
      "v_mul_f32 v60, v60, v7\n"
      "s_nop 0\n"
      "v_readlane_b32 s62, v60, 0\n"
      "v_readlane_b32 s63, v60, 16\n"
      "s_nop 0\n"
      "v_fma_f32 v24, v24, v7, -s62\n"
      "v_fma_f32 v32, v32, v7, -s62\n"
      "v_fma_f32 v40, v40, v7, -s62\n"
      "v_fma_f32 v28, v28, v7, -s62\n"
      "v_fma_f32 v26, v26, v7, -s63\n"
      "v_fma_f32 v34, v34, v7, -s63\n"
      "v_fma_f32 v42, v42, v7, -s63\n"
      "v_fma_f32 v30, v30, v7, -s63\n"
      "v_exp_f32 v24, v24\n"
      "v_exp_f32 v32, v32\n"
      "v_exp_f32 v40, v40\n"
      "v_exp_f32 v28, v28\n"
      "v_exp_f32 v26, v26\n"
      "v_exp_f32 v34, v34\n"
      "v_exp_f32 v42, v42\n"
      "v_exp_f32 v30, v30\n"
      "v_cvt_pk_bf16_f32 v56, v24, v32\n"
      "v_cvt_pk_bf16_f32 v57, v40, v28\n"
      "v_cvt_pk_bf16_f32 v58, v26, v34\n"
      "v_cvt_pk_bf16_f32 v59, v42, v30\n"
      "ds_write2_b32 v3, v56, v57 offset0:0 offset1:8\n"
      "ds_write2_b32 v3, v58, v59 offset0:32 offset1:40\n"
      "ds_read_b128 v[8:11], v4 offset:0\n"
      "ds_read_b128 v[12:15], v4 offset:256\n"
      "ds_read_b128 v[16:19], v4 offset:512\n"
      "ds_read_b128 v[20:23], v4 offset:768\n"
      "v_add_f32 v61, v24, v32\n"
      "v_add_f32 v40, v40, v28\n"
      "v_add_f32 v62, v26, v34\n"
      "v_add_f32 v42, v42, v30\n"
      "v_add_f32 v61, v61, v40\n"
      "v_add_f32 v62, v62, v42\n"
      "v_mov_b64 v[24:25], 0\n"
      "v_mov_b64 v[26:27], 0\n"
      "v_mov_b64 v[28:29], 0\n"
      "v_mov_b64 v[30:31], 0\n"
      "v_mov_b64 v[32:33], 0\n"
      "v_mov_b64 v[34:35], 0\n"
      "v_mov_b64 v[36:37], 0\n"
      "v_mov_b64 v[38:39], 0\n"
      "v_mov_b64 v[40:41], 0\n"
      "v_mov_b64 v[42:43], 0\n"
      "v_mov_b64 v[44:45], 0\n"
      "v_mov_b64 v[46:47], 0\n"
      "v_mov_b64 v[48:49], 0\n"
      "v_mov_b64 v[50:51], 0\n"
      "v_mov_b64 v[52:53], 0\n"
      "v_mov_b64 v[54:55], 0\n"
      "s_waitcnt lgkmcnt(3)\n"
      "v_smfmac_f32_16x16x64_bf16 v[24:27], v[8:11], a[192:199], v1\n"
      "v_smfmac_f32_16x16x64_bf16 v[28:31], v[8:11], a[200:207], v1\n"
      "v_permlane16_swap_b32 v61, v62\n"
      "v_smfmac_f32_16x16x64_bf16 v[32:35], v[8:11], a[208:215], v1\n"
      "v_smfmac_f32_16x16x64_bf16 v[36:39], v[8:11], a[216:223], v1\n"
      "v_add_f32 v61, v61, v62\n"
      "v_smfmac_f32_16x16x64_bf16 v[40:43], v[8:11], a[224:231], v1\n"
      "v_smfmac_f32_16x16x64_bf16 v[44:47], v[8:11], a[232:239], v1\n"
      "v_mov_b32 v62, v61\n"
      "v_smfmac_f32_16x16x64_bf16 v[48:51], v[8:11], a[240:247], v1\n"
      "v_smfmac_f32_16x16x64_bf16 v[52:55], v[8:11], a[248:255], v1\n"
      "v_permlane32_swap_b32 v61, v62\n"
      "s_waitcnt lgkmcnt(2)\n"
      "v_smfmac_f32_16x16x64_bf16 v[24:27], v[12:15], v[128:135], v1\n"
      "v_smfmac_f32_16x16x64_bf16 v[28:31], v[12:15], v[136:143], v1\n"
      "v_add_f32 v61, v61, v62\n"
      "v_smfmac_f32_16x16x64_bf16 v[32:35], v[12:15], v[144:151], v1\n"
      "v_smfmac_f32_16x16x64_bf16 v[36:39], v[12:15], v[152:159], v1\n"
      "v_add_f32_dpp v61, v61, v61 row_ror:8 row_mask:0xf bank_mask:0xf\n"
      "v_smfmac_f32_16x16x64_bf16 v[40:43], v[12:15], v[160:167], v1\n"
      "v_smfmac_f32_16x16x64_bf16 v[44:47], v[12:15], v[168:175], v1\n"
      "v_add_f32_dpp v61, v61, v61 row_ror:4 row_mask:0xf bank_mask:0xf\n"
      "v_smfmac_f32_16x16x64_bf16 v[48:51], v[12:15], v[176:183], v1\n"
      "v_smfmac_f32_16x16x64_bf16 v[52:55], v[12:15], v[184:191], v1\n"
      "v_add_f32_dpp v61, v61, v61 row_ror:2 row_mask:0xf bank_mask:0xf\n"
      "s_waitcnt lgkmcnt(1)\n"
      "v_smfmac_f32_16x16x64_bf16 v[24:27], v[16:19], v[192:199], v1\n"
      "v_smfmac_f32_16x16x64_bf16 v[28:31], v[16:19], v[200:207], v1\n"
      "v_add_f32_dpp v61, v61, v61 row_ror:1 row_mask:0xf bank_mask:0xf\n"
      "v_smfmac_f32_16x16x64_bf16 v[32:35], v[16:19], v[208:215], v1\n"
      "v_smfmac_f32_16x16x64_bf16 v[36:39], v[16:19], v[216:223], v1\n"
      "v_bfe_u32 v3, v0, 4, 1\n"
      "v_smfmac_f32_16x16x64_bf16 v[40:43], v[16:19], v[224:231], v1\n"
      "v_smfmac_f32_16x16x64_bf16 v[44:47], v[16:19], v[232:239], v1\n"
      "s_lshl_b32 s0, s44, 4\n"
      "s_add_u32 s0, s0, 161792\n"
      "v_lshl_add_u32 v3, v3, 3, s0\n"
      "v_smfmac_f32_16x16x64_bf16 v[48:51], v[16:19], v[240:247], v1\n"
      "v_smfmac_f32_16x16x64_bf16 v[52:55], v[16:19], v[248:255], v1\n"
      "s_cmp_eq_u32 s53, 0\n"
      "s_cbranch_scc0 .Lt_12_%=\n"
      "v_perm_b32 v5, v72, v64, s56\n"
      "v_perm_b32 v72, v72, v64, s57\n"
      "v_mov_b32 v64, v5\n"
      "v_perm_b32 v5, v73, v65, s56\n"
      "v_perm_b32 v6, v73, v65, s57\n"
      "v_perm_b32 v65, v88, v80, s56\n"
      "v_perm_b32 v73, v88, v80, s57\n"
      "v_mov_b32 v80, v5\n"
      "v_mov_b32 v88, v6\n"
      "v_perm_b32 v5, v74, v66, s56\n"
      "v_perm_b32 v6, v74, v66, s57\n"
      "v_perm_b32 v66, v104, v96, s56\n"
      "v_perm_b32 v74, v104, v96, s57\n"
      "v_mov_b32 v96, v5\n"
      "v_mov_b32 v104, v6\n"
      "v_perm_b32 v5, v75, v67, s56\n"
      "v_perm_b32 v6, v75, v67, s57\n"
      "v_perm_b32 v67, v120, v112, s56\n"
      "v_perm_b32 v75, v120, v112, s57\n"
      "v_mov_b32 v112, v5\n"
      "v_mov_b32 v120, v6\n"
      "v_perm_b32 v5, v89, v81, s56\n"
      "v_perm_b32 v89, v89, v81, s57\n"
      "v_mov_b32 v81, v5\n"
      "v_perm_b32 v5, v90, v82, s56\n"
      "v_perm_b32 v6, v90, v82, s57\n"
      "v_perm_b32 v82, v105, v97, s56\n"
      "v_perm_b32 v90, v105, v97, s57\n"
      "v_mov_b32 v97, v5\n"
      "v_mov_b32 v105, v6\n"
      "v_perm_b32 v5, v91, v83, s56\n"
      "v_perm_b32 v6, v91, v83, s57\n"
      "v_perm_b32 v83, v121, v113, s56\n"
      "v_perm_b32 v91, v121, v113, s57\n"
      "v_mov_b32 v113, v5\n"
      "v_mov_b32 v121, v6\n"
      "v_perm_b32 v5, v106, v98, s56\n"
      "v_perm_b32 v106, v106, v98, s57\n"
      "v_mov_b32 v98, v5\n"
      "v_perm_b32 v5, v107, v99, s56\n"
      "v_perm_b32 v6, v107, v99, s57\n"
      "v_perm_b32 v99, v122, v114, s56\n"
      "v_perm_b32 v107, v122, v114, s57\n"
      "v_mov_b32 v114, v5\n"
      "v_mov_b32 v122, v6\n"
      "v_perm_b32 v5, v123, v115, s56\n"
      "v_perm_b32 v123, v123, v115, s57\n"
      "v_mov_b32 v115, v5\n"
      "s_waitcnt vmcnt(0)\n"
      "s_lshr_b32 s0, s33, 5\n"
      "s_and_b32 s1, s44, 1\n"
      "s_lshl_b32 s1, s1, 1\n"
      "v_lshrrev_b32 v5, 3, v0\n"
      "v_mul_u32_u24 v5, s0, v5\n"
      "v_bfe_u32 v6, v0, 4, 1\n"
      "v_or_b32 v6, s1, v6\n"
      "v_lshlrev_b32 v6, 1, v6\n"
      "v_xor_b32 v6, v0, v6\n"
      "v_and_b32 v6, 7, v6\n"
      "v_lshl_add_u32 v5, v6, 4, v5\n"
      "s_mov_b32 m0, s35\n"
      "s_add_u32 s35, s35, 4096\n"
      "buffer_load_dwordx4 v5, s[28:31], s34 offen nt lds\n"
      "s_add_u32 s34, s34, s33\n"
      "s_mov_b32 m0, s35\n"
      "s_add_u32 s35, s35, 4096\n"
      "buffer_load_dwordx4 v5, s[28:31], s34 offen nt lds\n"
      "s_add_u32 s34, s34, s33\n"
      "s_mov_b32 m0, s35\n"
      "s_add_u32 s35, s35, 4096\n"
      "buffer_load_dwordx4 v5, s[28:31], s34 offen nt lds\n"
      "s_add_u32 s34, s34, s33\n"
      "s_mov_b32 m0, s35\n"
      "s_add_u32 s35, s35, 4096\n"
      "buffer_load_dwordx4 v5, s[28:31], s34 offen nt lds\n"
      "s_add_u32 s34, s34, s33\n"
      "s_cmp_eq_u32 s47, 0\n"
      "s_cbranch_scc0 .Le_13_%=\n"
      "s_mov_b32 m0, s35\n"
      "s_add_u32 s35, s35, 4096\n"
      "buffer_load_dwordx4 v5, s[28:31], s34 offen nt lds\n"
      "s_add_u32 s34, s34, s33\n"
      "s_mov_b32 m0, s35\n"
      "s_add_u32 s35, s35, 4096\n"
      "buffer_load_dwordx4 v5, s[28:31], s34 offen nt lds\n"
      "s_add_u32 s34, s34, s33\n"
      "s_mov_b32 m0, s35\n"
      "s_add_u32 s35, s35, 4096\n"
      "buffer_load_dwordx4 v5, s[28:31], s34 offen nt lds\n"
      "s_add_u32 s34, s34, s33\n"
      "s_mov_b32 m0, s35\n"
      "s_add_u32 s35, s35, 4096\n"
      "buffer_load_dwordx4 v5, s[28:31], s34 offen nt lds\n"
      "s_add_u32 s34, s34, s33\n"
      "s_mov_b32 m0, s35\n"
      "s_add_u32 s35, s35, 4096\n"
      "buffer_load_dwordx4 v5, s[28:31], s34 offen nt lds\n"
      "s_add_u32 s34, s34, s33\n"
      "s_mov_b32 m0, s35\n"
      "s_add_u32 s35, s35, 4096\n"
      "buffer_load_dwordx4 v5, s[28:31], s34 offen nt lds\n"
      "s_add_u32 s34, s34, s33\n"
      "s_mov_b32 m0, s35\n"
      "s_add_u32 s35, s35, 4096\n"
      "buffer_load_dwordx4 v5, s[28:31], s34 offen nt lds\n"
      "s_add_u32 s34, s34, s33\n"
      ".Le_13_%=:\n"
      "v_perm_b32 v5, v76, v68, s56\n"
      "v_perm_b32 v76, v76, v68, s57\n"
      "v_mov_b32 v68, v5\n"
      "v_perm_b32 v5, v77, v69, s56\n"
      "v_perm_b32 v6, v77, v69, s57\n"
      "v_perm_b32 v69, v92, v84, s56\n"
      "v_perm_b32 v77, v92, v84, s57\n"
      "v_mov_b32 v84, v5\n"
      "v_mov_b32 v92, v6\n"
      "v_perm_b32 v5, v78, v70, s56\n"
      "v_perm_b32 v6, v78, v70, s57\n"
      "v_perm_b32 v70, v108, v100, s56\n"
      "v_perm_b32 v78, v108, v100, s57\n"
      "v_mov_b32 v100, v5\n"
      "v_mov_b32 v108, v6\n"
      "v_perm_b32 v5, v79, v71, s56\n"
      "v_perm_b32 v6, v79, v71, s57\n"
      "v_perm_b32 v71, v124, v116, s56\n"
      "v_perm_b32 v79, v124, v116, s57\n"
      "v_mov_b32 v116, v5\n"
      "v_mov_b32 v124, v6\n"
      "v_perm_b32 v5, v93, v85, s56\n"
      "v_perm_b32 v93, v93, v85, s57\n"
      "v_mov_b32 v85, v5\n"
      "v_perm_b32 v5, v94, v86, s56\n"
      "v_perm_b32 v6, v94, v86, s57\n"
      "v_perm_b32 v86, v109, v101, s56\n"
      "v_perm_b32 v94, v109, v101, s57\n"
      "v_mov_b32 v101, v5\n"
      "v_mov_b32 v109, v6\n"
      "v_perm_b32 v5, v95, v87, s56\n"
      "v_perm_b32 v6, v95, v87, s57\n"
      "v_perm_b32 v87, v125, v117, s56\n"
      "v_perm_b32 v95, v125, v117, s57\n"
      "v_mov_b32 v117, v5\n"
      "v_mov_b32 v125, v6\n"
      "v_perm_b32 v5, v110, v102, s56\n"
      "v_perm_b32 v110, v110, v102, s57\n"
      "v_mov_b32 v102, v5\n"
      "v_perm_b32 v5, v111, v103, s56\n"
      "v_perm_b32 v6, v111, v103, s57\n"
      "v_perm_b32 v103, v126, v118, s56\n"
      "v_perm_b32 v111, v126, v118, s57\n"
      "v_mov_b32 v118, v5\n"
      "v_mov_b32 v126, v6\n"
      "v_perm_b32 v5, v127, v119, s56\n"
      "v_perm_b32 v127, v127, v119, s57\n"
      "v_mov_b32 v119, v5\n"
      ".Lt_12_%=:\n"
      "s_waitcnt lgkmcnt(0)\n"
      "v_smfmac_f32_16x16x64_bf16 v[24:27], v[20:23], v[64:71], v1\n"
      "v_smfmac_f32_16x16x64_bf16 v[28:31], v[20:23], v[72:79], v1\n"
      "v_smfmac_f32_16x16x64_bf16 v[32:35], v[20:23], v[80:87], v1\n"
      "v_smfmac_f32_16x16x64_bf16 v[36:39], v[20:23], v[88:95], v1\n"
      "v_smfmac_f32_16x16x64_bf16 v[40:43], v[20:23], v[96:103], v1\n"
      "v_smfmac_f32_16x16x64_bf16 v[44:47], v[20:23], v[104:111], v1\n"
      "v_smfmac_f32_16x16x64_bf16 v[48:51], v[20:23], v[112:119], v1\n"
      "v_smfmac_f32_16x16x64_bf16 v[52:55], v[20:23], v[120:127], v1\n"
      "s_nop 0\n"
      "v_add_f32_dpp v56, v24, v25 quad_perm:[0,1,2,3] row_mask:0x1 bank_mask:0xf\n"
      "v_add_f32_dpp v58, v26, v27 quad_perm:[0,1,2,3] row_mask:0x1 bank_mask:0xf\n"
      "v_add_f32_dpp v57, v28, v29 quad_perm:[0,1,2,3] row_mask:0x1 bank_mask:0xf\n"
      "v_add_f32_dpp v59, v30, v31 quad_perm:[0,1,2,3] row_mask:0x1 bank_mask:0xf\n"
      "v_add_f32_dpp v56, v32, v33 quad_perm:[0,1,2,3] row_mask:0x2 bank_mask:0xf\n"
      "v_add_f32_dpp v58, v34, v35 quad_perm:[0,1,2,3] row_mask:0x2 bank_mask:0xf\n"
      "v_add_f32_dpp v57, v36, v37 quad_perm:[0,1,2,3] row_mask:0x2 bank_mask:0xf\n"
      "v_add_f32_dpp v59, v38, v39 quad_perm:[0,1,2,3] row_mask:0x2 bank_mask:0xf\n"
      "v_add_f32_dpp v56, v40, v41 quad_perm:[0,1,2,3] row_mask:0x4 bank_mask:0xf\n"
      "v_add_f32_dpp v58, v42, v43 quad_perm:[0,1,2,3] row_mask:0x4 bank_mask:0xf\n"
      "v_add_f32_dpp v57, v44, v45 quad_perm:[0,1,2,3] row_mask:0x4 bank_mask:0xf\n"
      "v_add_f32_dpp v59, v46, v47 quad_perm:[0,1,2,3] row_mask:0x4 bank_mask:0xf\n"
      "v_add_f32_dpp v56, v48, v49 quad_perm:[0,1,2,3] row_mask:0x8 bank_mask:0xf\n"
      "v_add_f32_dpp v58, v50, v51 quad_perm:[0,1,2,3] row_mask:0x8 bank_mask:0xf\n"
      "v_add_f32_dpp v57, v52, v53 quad_perm:[0,1,2,3] row_mask:0x8 bank_mask:0xf\n"
      "v_add_f32_dpp v59, v54, v55 quad_perm:[0,1,2,3] row_mask:0x8 bank_mask:0xf\n"
      "ds_write_b64 v2, v[56:57]\n"
      "ds_write_b64 v2, v[58:59] offset:512\n"
      "s_mov_b64 exec, 0x10001\n"
      "ds_write_b64 v3, v[60:61]\n"
      "s_mov_b64 exec, -1\n"
      "s_lshl_b32 s0, s44, 8\n"
      "s_add_u32 s0, s0, 157696\n"
      "v_lshlrev_b32 v1, 2, v0\n"
      "v_add_u32 v2, s0, v1\n"
      "s_lshr_b32 s2, s44, 1\n"
      "s_lshl3_add_u32 s0, s2, 161792\n"
      "v_mov_b32 v3, s0\n"
      "s_lshl1_add_u32 s2, s45, s2\n"
      "s_lshl4_add_u32 s2, s2, s46\n"
      "s_lshl3_add_u32 s3, s2, s49\n"
      "s_lshl_b32 s2, s2, 9\n"
      "s_and_b32 s0, s44, 1\n"
      "s_lshl_b32 s0, s0, 8\n"
      "s_add_u32 s2, s2, s0\n"
      "s_add_u32 s2, s2, s49\n"
      "s_add_u32 s2, s2, 4096\n"
      "s_waitcnt lgkmcnt(0)\n"
      "s_barrier\n"
      "ds_read2_b64 v[12:15], v3 offset0:0 offset1:2\n"
      "ds_read2_b64 v[16:19], v3 offset0:4 offset1:6\n"
      "ds_read2st64_b32 v[8:9], v2 offset0:0 offset1:4\n"
      "ds_read2st64_b32 v[10:11], v2 offset0:8 offset1:12\n"
      "s_waitcnt lgkmcnt(2)\n"
      "v_max3_f32 v20, v12, v14, v16\n"
      "v_max_f32 v20, v20, v18\n"
      "v_sub_f32 v12, v12, v20\n"
      "v_sub_f32 v14, v14, v20\n"
      "v_sub_f32 v16, v16, v20\n"
      "v_sub_f32 v18, v18, v20\n"
      "v_exp_f32 v12, v12\n"
      "v_exp_f32 v14, v14\n"
      "v_exp_f32 v16, v16\n"
      "v_exp_f32 v18, v18\n"
      "s_waitcnt lgkmcnt(0)\n"
      "v_mul_f32 v8, v8, v12\n"
      "v_mul_f32 v21, v13, v12\n"
      "v_fmac_f32 v8, v9, v14\n"
      "v_fmac_f32 v21, v15, v14\n"
      "v_fmac_f32 v8, v10, v16\n"
      "v_fmac_f32 v21, v17, v16\n"
      "v_fmac_f32 v8, v11, v18\n"
      "v_fmac_f32 v21, v19, v18\n"
      "v_rcp_f32 v22, v21\n"
      "v_log_f32 v21, v21\n"
      "v_and_b32 v23, 28, v1\n"
      "v_mul_f32 v8, v8, v22\n"
      "v_add_f32 v20, v20, v21\n"
      "v_lshrrev_b32 v20, v23, v20\n"
      "v_bfi_b32 v8, 15, v20, v8\n"
      "v_min_u32 v8, -2, v8\n"
      "s_nop 1\n"
      "v_mov_b32_dpp v9, v8 quad_perm:[1,1,1,1] row_mask:0xf bank_mask:0xf\n"
      "v_mov_b32_dpp v10, v8 quad_perm:[2,2,2,2] row_mask:0xf bank_mask:0xf\n"
      "v_mov_b32_dpp v11, v8 quad_perm:[3,3,3,3] row_mask:0xf bank_mask:0xf\n"
      "s_mov_b32 exec_lo, 0x11111111\n"
      "s_mov_b32 exec_hi, 0x11111111\n"
      "buffer_store_dwordx4 v[8:11], v1, s[16:19], s2 offen sc1\n"
      "s_mov_b64 exec, -1\n"
      "v_and_b32 v4, 3, v0\n"
      "v_bfe_u32 v5, v0, 2, 2\n"
      "v_lshrrev_b32 v6, 4, v0\n"
      "v_lshl_add_u32 v2, v4, 2, v5\n"
      "v_lshlrev_b32 v1, 9, v2\n"
      "v_lshl_add_u32 v1, v6, 5, v1\n"
      "v_lshlrev_b32 v2, 3, v2\n"
      "s_lshl1_add_u32 s64, s45, s7\n"
      "s_lshl_b32 s0, s64, 13\n"
      "s_lshl_b32 s1, s44, 7\n"
      "s_add_u32 s0, s0, s1\n"
      "s_add_u32 s0, s0, s49\n"
      "s_add_u32 s0, s0, 4096\n"
      "s_lshl_b32 s1, s64, 7\n"
      "s_add_u32 s1, s1, s49\n"
      "s_mul_i32 s65, s44, 4608\n"
      "s_add_u32 s65, s65, s32\n"
      "v_lshl_add_u32 v3, v6, 3, v5\n"
      "v_mul_u32_u24 v3, 144, v3\n"
      "v_lshl_add_u32 v3, v4, 3, v3\n"
      "v_add_u32 v3, s65, v3\n"
      "v_and_b32 v7, 32, v0\n"
      "v_lshlrev_b32 v7, 2, v7\n"
      "v_lshl_add_u32 v7, v4, 5, v7\n"
      "v_and_or_b32 v7, v0, 16, v7\n"
      "v_add_u32 v7, s65, v7\n"
      "v_lshrrev_b32 v5, 3, v0\n"
      "v_and_b32 v4, 7, v0\n"
      "v_lshlrev_b32 v4, 4, v4\n"
      "v_lshl_add_u32 v6, v5, 14, v4\n"
      "v_lshl_add_u32 v4, v5, 9, v4\n"
      "s_lshl_b32 s65, s44, 7\n"
      "s_add_u32 s65, s65, s32\n"
      "v_add_u32 v4, s65, v4\n"
      "s_and_b32 s65, s46, 7\n"
      "s_mul_i32 s65, s65, 288\n"
      "s_add_u32 s65, s65, s64\n"
      "s_lshl_b32 s65, s65, 7\n"
      "s_lshl_b32 s66, s44, 12\n"
      "s_add_u32 s65, s65, s66\n"
      "s_add_u32 s65, s65, s49\n"
      "s_add_u32 s65, s65, 266240\n"
      "s_cmp_eq_u32 s44, 0\n"
      "s_cselect_b32 s66, 24, 16\n"
      "s_bfm_b32 s66, s66, 0\n"
      "s_mov_b32 s67, 0\n"
      "s_sleep 2\n"
      "s_mov_b32 s54, 262144\n"
      "buffer_load_dwordx4 v[8:11], v1, s[16:19], s0 offen sc1\n"
      "buffer_load_dwordx4 v[12:15], v1, s[16:19], s0 offen offset:16 sc1\n"
      "s_waitcnt vmcnt(2)\n"
      "ds_read_b64_tr_b16 v[24:25], v3 offset:0\n"
      "ds_read_b64_tr_b16 v[26:27], v3 offset:576\n"
      "ds_read_b64_tr_b16 v[28:29], v3 offset:32\n"
      "ds_read_b64_tr_b16 v[30:31], v3 offset:608\n"
      "ds_read_b64_tr_b16 v[32:33], v3 offset:64\n"
      "ds_read_b64_tr_b16 v[34:35], v3 offset:640\n"
      "ds_read_b64_tr_b16 v[36:37], v3 offset:96\n"
      "ds_read_b64_tr_b16 v[38:39], v3 offset:672\n"
      "ds_read_b64_tr_b16 v[40:41], v3 offset:128\n"
      "ds_read_b64_tr_b16 v[42:43], v3 offset:704\n"
      ".Lchk_14_%=:\n"
      "s_waitcnt vmcnt(0)\n"
      "v_max3_u32 v18, v8, v9, v10\n"
      "v_max3_u32 v18, v18, v11, v12\n"
      "v_max3_u32 v18, v18, v13, v14\n"
      "v_max_u32 v18, v18, v15\n"
      "v_cmp_eq_u32 vcc, -1, v18\n"
      "s_cbranch_vccz .Ldone_15_%=\n"
      "s_sub_u32 s54, s54, 1\n"
      "s_cbranch_scc1 .Ldone_15_%=\n"
      "buffer_load_dwordx4 v[8:11], v1, s[16:19], s0 offen sc1\n"
      "buffer_load_dwordx4 v[12:15], v1, s[16:19], s0 offen offset:16 sc1\n"
      "s_branch .Lchk_14_%=\n"
      ".Ldone_15_%=:\n"
      "v_alignbit_b32 v16, v8, v16, 4\n"
      "v_alignbit_b32 v16, v9, v16, 4\n"
      "v_alignbit_b32 v16, v10, v16, 4\n"
      "v_alignbit_b32 v16, v11, v16, 4\n"
      "v_alignbit_b32 v16, v12, v16, 4\n"
      "v_alignbit_b32 v16, v13, v16, 4\n"
      "v_alignbit_b32 v16, v14, v16, 4\n"
      "v_alignbit_b32 v16, v15, v16, 4\n"
      "s_nop 1\n"
      "v_max_f32_dpp v18, v16, v16 row_ror:4 row_mask:0xf bank_mask:0xf\n"
      "s_nop 1\n"
      "v_max_f32_dpp v18, v18, v18 row_ror:8 row_mask:0xf bank_mask:0xf\n"
      "v_sub_f32 v16, v16, v18\n"
      "v_exp_f32 v16, v16\n"
      "s_nop 1\n"
      "v_add_f32_dpp v17, v16, v16 row_ror:4 row_mask:0xf bank_mask:0xf\n"
      "s_nop 1\n"
      "v_add_f32_dpp v17, v17, v17 row_ror:8 row_mask:0xf bank_mask:0xf\n"
      "v_rcp_f32 v17, v17\n"
      "s_nop 0\n"
      "v_mul_f32 v16, v16, v17\n"
      "v_pk_mul_f32 v[8:9], v[8:9], v[16:17] op_sel_hi:[1,0]\n"
      "v_pk_mul_f32 v[10:11], v[10:11], v[16:17] op_sel_hi:[1,0]\n"
      "v_pk_mul_f32 v[12:13], v[12:13], v[16:17] op_sel_hi:[1,0]\n"
      "v_pk_mul_f32 v[14:15], v[14:15], v[16:17] op_sel_hi:[1,0]\n"
      "v_add_f32_dpp v8, v8, v8 row_ror:4 row_mask:0xf bank_mask:0xf\n"
      "v_add_f32_dpp v9, v9, v9 row_ror:4 row_mask:0xf bank_mask:0xf\n"
      "v_add_f32_dpp v10, v10, v10 row_ror:4 row_mask:0xf bank_mask:0xf\n"
      "v_add_f32_dpp v11, v11, v11 row_ror:4 row_mask:0xf bank_mask:0xf\n"
      "v_add_f32_dpp v12, v12, v12 row_ror:4 row_mask:0xf bank_mask:0xf\n"
      "v_add_f32_dpp v13, v13, v13 row_ror:4 row_mask:0xf bank_mask:0xf\n"
      "v_add_f32_dpp v14, v14, v14 row_ror:4 row_mask:0xf bank_mask:0xf\n"
      "v_add_f32_dpp v15, v15, v15 row_ror:4 row_mask:0xf bank_mask:0xf\n"
      "v_add_f32_dpp v8, v8, v8 row_ror:8 row_mask:0xf bank_mask:0xf\n"
      "v_add_f32_dpp v9, v9, v9 row_ror:8 row_mask:0xf bank_mask:0xf\n"
      "v_add_f32_dpp v10, v10, v10 row_ror:8 row_mask:0xf bank_mask:0xf\n"
      "v_add_f32_dpp v11, v11, v11 row_ror:8 row_mask:0xf bank_mask:0xf\n"
      "v_add_f32_dpp v12, v12, v12 row_ror:8 row_mask:0xf bank_mask:0xf\n"
      "v_add_f32_dpp v13, v13, v13 row_ror:8 row_mask:0xf bank_mask:0xf\n"
      "v_add_f32_dpp v14, v14, v14 row_ror:8 row_mask:0xf bank_mask:0xf\n"
      "v_add_f32_dpp v15, v15, v15 row_ror:8 row_mask:0xf bank_mask:0xf\n"
      "v_cvt_pk_bf16_f32 v8, v8, v9\n"
      "v_cvt_pk_bf16_f32 v9, v10, v11\n"
      "v_cvt_pk_bf16_f32 v10, v12, v13\n"
      "v_cvt_pk_bf16_f32 v11, v14, v15\n"
      "s_waitcnt lgkmcnt(8)\n"
      "s_nop 0\n"
      "v_mfma_f32_16x16x32_bf16 v[44:47], v[24:27], v[8:11], 0\n"
      "s_waitcnt lgkmcnt(6)\n"
      "v_mfma_f32_16x16x32_bf16 v[48:51], v[28:31], v[8:11], 0\n"
      "s_waitcnt lgkmcnt(4)\n"
      "v_mfma_f32_16x16x32_bf16 v[52:55], v[32:35], v[8:11], 0\n"
      "s_waitcnt lgkmcnt(2)\n"
      "v_mfma_f32_16x16x32_bf16 v[56:59], v[36:39], v[8:11], 0\n"
      "s_waitcnt lgkmcnt(0)\n"
      "v_mfma_f32_16x16x32_bf16 v[60:63], v[40:43], v[8:11], 0\n"
      "s_mov_b32 exec_lo, 0x000f000f\n"
      "s_mov_b32 exec_hi, 0x000f000f\n"
      "ds_write_b128 v7, v[44:47] offset:0\n"
      "ds_write_b128 v7, v[48:51] offset:256\n"
      "ds_write_b128 v7, v[52:55] offset:512\n"
      "s_nop 0\n"
      "ds_write_b128 v7, v[56:59] offset:768\n"
      "s_nop 0\n"
      "ds_write_b128 v7, v[60:63] offset:1024\n"
      "s_mov_b64 exec, s[66:67]\n"
      "s_waitcnt lgkmcnt(0)\n"
      "s_barrier\n"
      "ds_read_b128 v[8:11], v4 offset:0\n"
      "ds_read_b128 v[12:15], v4 offset:4608\n"
      "ds_read_b128 v[16:19], v4 offset:9216\n"
      "ds_read_b128 v[20:23], v4 offset:13824\n"
      "s_waitcnt lgkmcnt(2)\n"
      "v_pk_add_f32 v[8:9], v[8:9], v[12:13]\n"
      "v_pk_add_f32 v[10:11], v[10:11], v[14:15]\n"
      "s_waitcnt lgkmcnt(0)\n"
      "v_pk_add_f32 v[16:17], v[16:17], v[20:21]\n"
      "v_pk_add_f32 v[18:19], v[18:19], v[22:23]\n"
      "v_pk_add_f32 v[8:9], v[8:9], v[16:17]\n"
      "v_pk_add_f32 v[10:11], v[10:11], v[18:19]\n"
      "v_min_u32 v8, -2, v8\n"
      "v_min_u32 v9, -2, v9\n"
      "v_min_u32 v10, -2, v10\n"
      "v_min_u32 v11, -2, v11\n"
      "buffer_store_dwordx4 v[8:11], v6, s[16:19], s65 offen sc1\n"
      "s_mov_b64 exec, -1\n"
      "s_cmp_eq_u32 s47, 1\n"
      "s_cbranch_scc1 .Ldown_8_%=\n"
      "s_cmp_eq_u32 s47, 2\n"
      "s_cbranch_scc1 .Lidle_9_%=\n"
      "s_cmp_eq_u32 s53, 0\n"
      "s_cbranch_scc0 .Ll0_16_%=\n"
      "s_barrier\n"
      "s_lshr_b32 s0, s33, 5\n"
      "s_and_b32 s1, s44, 1\n"
      "s_lshl_b32 s1, s1, 1\n"
      "v_lshrrev_b32 v1, 3, v0\n"
      "v_mul_u32_u24 v1, s0, v1\n"
      "v_bfe_u32 v2, v0, 4, 1\n"
      "v_or_b32 v2, s1, v2\n"
      "v_lshlrev_b32 v2, 1, v2\n"
      "v_xor_b32 v2, v0, v2\n"
      "v_and_b32 v2, 7, v2\n"
      "v_lshl_add_u32 v1, v2, 4, v1\n"
      "s_mov_b32 m0, s35\n"
      "s_add_u32 s35, s35, 4096\n"
      "buffer_load_dwordx4 v1, s[28:31], s34 offen nt lds\n"
      "s_add_u32 s34, s34, s33\n"
      "s_mov_b32 m0, s35\n"
      "s_add_u32 s35, s35, 4096\n"
      "buffer_load_dwordx4 v1, s[28:31], s34 offen nt lds\n"
      "s_add_u32 s34, s34, s33\n"
      "s_mov_b32 m0, s35\n"
      "s_add_u32 s35, s35, 4096\n"
      "buffer_load_dwordx4 v1, s[28:31], s34 offen nt lds\n"
      "s_add_u32 s34, s34, s33\n"
      "s_mov_b32 m0, s35\n"
      "s_add_u32 s35, s35, 4096\n"
      "buffer_load_dwordx4 v1, s[28:31], s34 offen nt lds\n"
      "s_add_u32 s34, s34, s33\n"
      "s_mov_b32 m0, s35\n"
      "s_add_u32 s35, s35, 4096\n"
      "buffer_load_dwordx4 v1, s[28:31], s34 offen nt lds\n"
      "s_add_u32 s34, s34, s33\n"
      "s_mov_b32 m0, s35\n"
      "s_add_u32 s35, s35, 4096\n"
      "buffer_load_dwordx4 v1, s[28:31], s34 offen nt lds\n"
      "s_add_u32 s34, s34, s33\n"
      "s_mov_b32 m0, s35\n"
      "s_add_u32 s35, s35, 4096\n"
      "buffer_load_dwordx4 v1, s[28:31], s34 offen nt lds\n"
      "s_add_u32 s34, s34, s33\n"
      "s_waitcnt vmcnt(0)\n"
      "s_barrier\n"
      ".Ll0_16_%=:\n"
      "v_bfe_u32 v5, v0, 2, 2\n"
      "v_lshrrev_b32 v6, 4, v0\n"
      "v_and_b32 v7, 3, v0\n"
      "v_lshl_add_u32 v3, v6, 3, v5\n"
      "v_lshlrev_b32 v3, 7, v3\n"
      "v_lshl_add_u32 v3, v7, 3, v3\n"
      "v_lshrrev_b32 v4, 1, v5\n"
      "v_and_b32 v1, 1, v6\n"
      "v_lshl_add_u32 v4, v1, 1, v4\n"
      "v_xor_b32 v4, s44, v4\n"
      "v_lshl_add_u32 v3, v4, 5, v3\n"
      "v_add_u32 v3, 65536, v3\n"
      "v_and_b32 v4, 2, v6\n"
      "v_lshlrev_b32 v4, 10, v4\n"
      "v_sub_u32 v4, v3, v4\n"
      "v_add_u32 v1, 36864, v3\n"
      "v_add_u32 v2, 36864, v4\n"
      "ds_read_b64_tr_b16 v[48:49], v3 offset:0\n"
      "ds_read_b64_tr_b16 v[50:51], v3 offset:512\n"
      "ds_read_b64_tr_b16 v[52:53], v3 offset:18432\n"
      "ds_read_b64_tr_b16 v[54:55], v3 offset:18944\n"
      "ds_read_b64_tr_b16 v[56:57], v1 offset:0\n"
      "ds_read_b64_tr_b16 v[58:59], v1 offset:512\n"
      "ds_read_b64_tr_b16 v[60:61], v1 offset:18432\n"
      "ds_read_b64_tr_b16 v[62:63], v1 offset:18944\n"
      "ds_read_b64_tr_b16 v[36:37], v3 offset:4096\n"
      "ds_read_b64_tr_b16 v[38:39], v3 offset:4608\n"
      "ds_read_b64_tr_b16 v[40:41], v3 offset:22528\n"
      "ds_read_b64_tr_b16 v[42:43], v3 offset:23040\n"
      "ds_read_b64_tr_b16 v[8:9], v1 offset:4096\n"
      "ds_read_b64_tr_b16 v[10:11], v1 offset:4608\n"
      "ds_read_b64_tr_b16 v[12:13], v1 offset:22528\n"
      "ds_read_b64_tr_b16 v[14:15], v1 offset:23040\n"
      "ds_read_b64_tr_b16 v[16:17], v3 offset:8192\n"
      "ds_read_b64_tr_b16 v[18:19], v3 offset:8704\n"
      "ds_read_b64_tr_b16 v[20:21], v3 offset:26624\n"
      "ds_read_b64_tr_b16 v[22:23], v3 offset:27136\n"
      "v_mov_b32 v7, 0\n"
      "v_cmp_gt_u32 vcc, 32, v0\n"
      "v_mov_b32 v28, s58\n"
      "v_mov_b32 v29, s58\n"
      "v_mov_b32 v30, s58\n"
      "v_mov_b32 v31, s58\n"
      "v_cndmask_b32 v32, v7, v28, vcc\n"
      "v_cndmask_b32 v33, v7, v28, vcc\n"
      "v_cndmask_b32 v34, v7, v28, vcc\n"
      "v_cndmask_b32 v35, v7, v28, vcc\n"
      "s_waitcnt lgkmcnt(15)\n"
      "v_mfma_f32_16x16x32_bf16 v[44:47], v[48:51], v[28:31], 0\n"
      "ds_read_b64_tr_b16 v[48:49], v1 offset:8192\n"
      "ds_read_b64_tr_b16 v[50:51], v1 offset:8704\n"
      "v_mfma_f32_16x16x32_bf16 v[44:47], v[52:55], v[28:31], v[44:47]\n"
      "ds_read_b64_tr_b16 v[52:53], v1 offset:26624\n"
      "ds_read_b64_tr_b16 v[54:55], v1 offset:27136\n"
      "s_waitcnt lgkmcnt(15)\n"
      "v_mfma_f32_16x16x32_bf16 v[44:47], v[56:59], v[28:31], v[44:47]\n"
      "ds_read_b64_tr_b16 v[56:57], v3 offset:12288\n"
      "ds_read_b64_tr_b16 v[58:59], v3 offset:12800\n"
      "v_mfma_f32_16x16x32_bf16 v[44:47], v[60:63], v[28:31], v[44:47]\n"
      "ds_read_b64_tr_b16 v[60:61], v3 offset:30720\n"
      "ds_read_b64_tr_b16 v[62:63], v3 offset:31232\n"
      "s_waitcnt lgkmcnt(15)\n"
      "v_mfma_f32_16x16x32_bf16 v[44:47], v[36:39], v[28:31], v[44:47]\n"
      "ds_read_b64_tr_b16 v[36:37], v1 offset:12288\n"
      "ds_read_b64_tr_b16 v[38:39], v1 offset:12800\n"
      "v_mfma_f32_16x16x32_bf16 v[44:47], v[40:43], v[28:31], v[44:47]\n"
      "ds_read_b64_tr_b16 v[40:41], v1 offset:30720\n"
      "ds_read_b64_tr_b16 v[42:43], v1 offset:31232\n"
      "s_waitcnt lgkmcnt(15)\n"
      "v_mfma_f32_16x16x32_bf16 v[44:47], v[8:11], v[28:31], v[44:47]\n"
      "ds_read_b64_tr_b16 v[8:9], v4 offset:16384\n"
      "ds_read_b64_tr_b16 v[10:11], v4 offset:16896\n"
      "v_mfma_f32_16x16x32_bf16 v[44:47], v[12:15], v[28:31], v[44:47]\n"
      "ds_read_b64_tr_b16 v[12:13], v4 offset:34816\n"
      "ds_read_b64_tr_b16 v[14:15], v4 offset:35328\n"
      "s_waitcnt lgkmcnt(15)\n"
      "v_mfma_f32_16x16x32_bf16 v[44:47], v[16:19], v[28:31], v[44:47]\n"
      "ds_read_b64_tr_b16 v[16:17], v2 offset:16384\n"
      "ds_read_b64_tr_b16 v[18:19], v2 offset:16896\n"
      "v_mfma_f32_16x16x32_bf16 v[44:47], v[20:23], v[28:31], v[44:47]\n"
      "ds_read_b64_tr_b16 v[20:21], v2 offset:34816\n"
      "ds_read_b64_tr_b16 v[22:23], v2 offset:35328\n"
      "s_waitcnt lgkmcnt(15)\n"
      "v_mfma_f32_16x16x32_bf16 v[44:47], v[48:51], v[28:31], v[44:47]\n"
      "v_mfma_f32_16x16x32_bf16 v[44:47], v[52:55], v[28:31], v[44:47]\n"
      "s_waitcnt lgkmcnt(14)\n"
      "v_mfma_f32_16x16x32_bf16 v[44:47], v[56:59], v[28:31], v[44:47]\n"
      "s_waitcnt lgkmcnt(12)\n"
      "v_mfma_f32_16x16x32_bf16 v[44:47], v[60:63], v[28:31], v[44:47]\n"
      "s_waitcnt lgkmcnt(10)\n"
      "v_mfma_f32_16x16x32_bf16 v[44:47], v[36:39], v[28:31], v[44:47]\n"
      "s_waitcnt lgkmcnt(8)\n"
      "v_mfma_f32_16x16x32_bf16 v[44:47], v[40:43], v[28:31], v[44:47]\n"
      "s_waitcnt lgkmcnt(6)\n"
      "v_mfma_f32_16x16x32_bf16 v[44:47], v[8:11], v[32:35], v[44:47]\n"
      "s_waitcnt lgkmcnt(4)\n"
      "v_mfma_f32_16x16x32_bf16 v[44:47], v[12:15], v[32:35], v[44:47]\n"
      "s_waitcnt lgkmcnt(2)\n"
      "v_mfma_f32_16x16x32_bf16 v[44:47], v[16:19], v[32:35], v[44:47]\n"
      "s_waitcnt lgkmcnt(0)\n"
      "v_mfma_f32_16x16x32_bf16 v[44:47], v[20:23], v[32:35], v[44:47]\n"
      "s_mul_i32 s0, s44, 1216\n"
      "s_add_u32 s0, s0, 157696\n"
      "v_lshlrev_b32 v28, 4, v0\n"
      "v_add_u32 v30, s0, v28\n"
      "v_lshlrev_b32 v29, 2, v0\n"
      "v_add_u32 v29, 0x400, v29\n"
      "v_add_u32 v32, s0, v29\n"
      "v_mov_b32 v33, 0x40000000\n"
      "v_cmp_gt_u32 vcc, 32, v0\n"
      "s_nop 1\n"
      "v_cndmask_b32 v29, v33, v29, vcc\n"
      "v_bfe_u32 v33, v0, 2, 2\n"
      "v_mul_u32_u24 v31, 1216, v33\n"
      "v_lshrrev_b32 v33, 4, v0\n"
      "v_lshl_add_u32 v31, v33, 6, v31\n"
      "v_and_b32 v33, 3, v0\n"
      "v_lshl_add_u32 v31, v33, 4, v31\n"
      "v_add_u32 v31, 157696, v31\n"
      "v_mov_b64 v[24:25], 0\n"
      "v_mov_b64 v[26:27], 0\n"
      "v_and_b32 v7, 3, v0\n"
      "v_lshlrev_b32 v7, 4, v7\n"
      "v_bfe_u32 v5, v0, 4, 0\n"
      "v_lshl_add_u32 v7, v5, 3, v7\n"
      "v_lshrrev_b32 v5, 5, v0\n"
      "v_lshl_add_u32 v7, v5, 6, v7\n"
      "s_bfe_u32 s0, s6, 0x30000\n"
      "s_lshl2_add_u32 s0, s0, s44\n"
      "s_mul_i32 s1, s0, 1152\n"
      "s_add_u32 s1, s1, s49\n"
      "s_add_u32 s1, s1, 561152\n"
      "s_lshr_b32 s64, s48, 1\n"
      "s_lshl2_add_u32 s64, s64, s44\n"
      "s_lshl_b32 s64, s64, 7\n"
      "s_and_b32 s0, s48, 1\n"
      "s_mul_i32 s0, s0, 36864\n"
      "s_add_u32 s64, s64, s0\n"
      "s_add_u32 s64, s64, s49\n"
      "s_add_u32 s64, s64, 598016\n"
      "ds_read_b64_tr_b16 v[48:49], v3 offset:0\n"
      "ds_read_b64_tr_b16 v[50:51], v3 offset:512\n"
      "ds_read_b64_tr_b16 v[52:53], v3 offset:18432\n"
      "ds_read_b64_tr_b16 v[54:55], v3 offset:18944\n"
      "ds_read_b64_tr_b16 v[56:57], v1 offset:0\n"
      "ds_read_b64_tr_b16 v[58:59], v1 offset:512\n"
      "ds_read_b64_tr_b16 v[60:61], v1 offset:18432\n"
      "ds_read_b64_tr_b16 v[62:63], v1 offset:18944\n"
      "s_cmp_eq_u32 s53, 0\n"
      "s_cbranch_scc1 .Ls_17_%=\n"
      ".Ls_17_%=:\n"
      "s_sleep 8\n"
      "s_mov_b32 s54, 262144\n"
      "buffer_load_dwordx4 v[8:11], v28, s[16:19], s1 offen sc1\n"
      "buffer_load_dword v12, v29, s[16:19], s1 offen sc1\n"
      ".Lchk_18_%=:\n"
      "s_waitcnt vmcnt(0)\n"
      "v_max3_u32 v5, v8, v9, v10\n"
      "v_max3_u32 v5, v5, v11, v12\n"
      "v_cmp_eq_u32 vcc, -1, v5\n"
      "s_cbranch_vccz .Ldone_19_%=\n"
      "s_sub_u32 s54, s54, 1\n"
      "s_cbranch_scc1 .Ldone_19_%=\n"
      "buffer_load_dwordx4 v[8:11], v28, s[16:19], s1 offen sc1\n"
      "buffer_load_dword v12, v29, s[16:19], s1 offen sc1\n"
      "s_branch .Lchk_18_%=\n"
      ".Ldone_19_%=:\n"
      "ds_write_b128 v30, v[8:11]\n"
      "s_mov_b32 exec_hi, 0\n"
      "ds_write_b32 v32, v12\n"
      "s_waitcnt lgkmcnt(0)\n"
      "s_barrier\n"
      "ds_read_b128 v[24:27], v31 offset:1024\n"
      "s_mov_b64 exec, -1\n"
      "ds_read_b128 v[8:11], v31 offset:0\n"
      "ds_read_b128 v[12:15], v31 offset:256\n"
      "ds_read_b128 v[16:19], v31 offset:512\n"
      "ds_read_b128 v[20:23], v31 offset:768\n"
      "s_waitcnt lgkmcnt(3)\n"
      "v_dot2_f32_bf16 v5, v8, s58, 0\n"
      "v_dot2_f32_bf16 v6, v8, v8, 0\n"
      "v_dot2_f32_bf16 v5, v9, s58, v5\n"
      "v_dot2_f32_bf16 v6, v9, v9, v6\n"
      "v_dot2_f32_bf16 v5, v10, s58, v5\n"
      "v_dot2_f32_bf16 v6, v10, v10, v6\n"
      "v_dot2_f32_bf16 v5, v11, s58, v5\n"
      "v_dot2_f32_bf16 v6, v11, v11, v6\n"
      "v_mfma_f32_16x16x32_bf16 v[28:31], v[48:51], v[8:11], 0\n"
      "s_waitcnt lgkmcnt(2)\n"
      "v_dot2_f32_bf16 v5, v12, s58, v5\n"
      "v_dot2_f32_bf16 v6, v12, v12, v6\n"
      "ds_read_b64_tr_b16 v[48:49], v3 offset:4096\n"
      "ds_read_b64_tr_b16 v[50:51], v3 offset:4608\n"
      "v_mfma_f32_16x16x32_bf16 v[32:35], v[52:55], v[8:11], 0\n"
      "v_dot2_f32_bf16 v5, v13, s58, v5\n"
      "v_dot2_f32_bf16 v6, v13, v13, v6\n"
      "ds_read_b64_tr_b16 v[52:53], v3 offset:22528\n"
      "ds_read_b64_tr_b16 v[54:55], v3 offset:23040\n"
      "v_mfma_f32_16x16x32_bf16 v[36:39], v[56:59], v[8:11], 0\n"
      "v_dot2_f32_bf16 v5, v14, s58, v5\n"
      "v_dot2_f32_bf16 v6, v14, v14, v6\n"
      "ds_read_b64_tr_b16 v[56:57], v1 offset:4096\n"
      "ds_read_b64_tr_b16 v[58:59], v1 offset:4608\n"
      "v_mfma_f32_16x16x32_bf16 v[40:43], v[60:63], v[8:11], 0\n"
      "v_dot2_f32_bf16 v5, v15, s58, v5\n"
      "v_dot2_f32_bf16 v6, v15, v15, v6\n"
      "ds_read_b64_tr_b16 v[60:61], v1 offset:22528\n"
      "ds_read_b64_tr_b16 v[62:63], v1 offset:23040\n"
      "ds_read_b64_tr_b16 v[8:9], v3 offset:8192\n"
      "ds_read_b64_tr_b16 v[10:11], v3 offset:8704\n"
      "s_waitcnt lgkmcnt(8)\n"
      "v_mfma_f32_16x16x32_bf16 v[28:31], v[48:51], v[12:15], v[28:31]\n"
      "v_dot2_f32_bf16 v5, v16, s58, v5\n"
      "v_dot2_f32_bf16 v6, v16, v16, v6\n"
      "ds_read_b64_tr_b16 v[48:49], v3 offset:26624\n"
      "ds_read_b64_tr_b16 v[50:51], v3 offset:27136\n"
      "s_waitcnt lgkmcnt(8)\n"
      "v_mfma_f32_16x16x32_bf16 v[32:35], v[52:55], v[12:15], v[32:35]\n"
      "v_dot2_f32_bf16 v5, v17, s58, v5\n"
      "v_dot2_f32_bf16 v6, v17, v17, v6\n"
      "ds_read_b64_tr_b16 v[52:53], v1 offset:8192\n"
      "ds_read_b64_tr_b16 v[54:55], v1 offset:8704\n"
      "s_waitcnt lgkmcnt(8)\n"
      "v_mfma_f32_16x16x32_bf16 v[36:39], v[56:59], v[12:15], v[36:39]\n"
      "v_dot2_f32_bf16 v5, v18, s58, v5\n"
      "v_dot2_f32_bf16 v6, v18, v18, v6\n"
      "ds_read_b64_tr_b16 v[56:57], v1 offset:26624\n"
      "ds_read_b64_tr_b16 v[58:59], v1 offset:27136\n"
      "s_waitcnt lgkmcnt(8)\n"
      "v_mfma_f32_16x16x32_bf16 v[40:43], v[60:63], v[12:15], v[40:43]\n"
      "v_dot2_f32_bf16 v5, v19, s58, v5\n"
      "v_dot2_f32_bf16 v6, v19, v19, v6\n"
      "ds_read_b64_tr_b16 v[60:61], v3 offset:12288\n"
      "ds_read_b64_tr_b16 v[62:63], v3 offset:12800\n"
      "ds_read_b64_tr_b16 v[12:13], v3 offset:30720\n"
      "ds_read_b64_tr_b16 v[14:15], v3 offset:31232\n"
      "s_waitcnt lgkmcnt(10)\n"
      "v_mfma_f32_16x16x32_bf16 v[28:31], v[8:11], v[16:19], v[28:31]\n"
      "v_dot2_f32_bf16 v5, v20, s58, v5\n"
      "v_dot2_f32_bf16 v6, v20, v20, v6\n"
      "ds_read_b64_tr_b16 v[8:9], v1 offset:12288\n"
      "ds_read_b64_tr_b16 v[10:11], v1 offset:12800\n"
      "s_waitcnt lgkmcnt(10)\n"
      "v_mfma_f32_16x16x32_bf16 v[32:35], v[48:51], v[16:19], v[32:35]\n"
      "v_dot2_f32_bf16 v5, v21, s58, v5\n"
      "v_dot2_f32_bf16 v6, v21, v21, v6\n"
      "ds_read_b64_tr_b16 v[48:49], v1 offset:30720\n"
      "ds_read_b64_tr_b16 v[50:51], v1 offset:31232\n"
      "s_waitcnt lgkmcnt(10)\n"
      "v_mfma_f32_16x16x32_bf16 v[36:39], v[52:55], v[16:19], v[36:39]\n"
      "v_dot2_f32_bf16 v5, v22, s58, v5\n"
      "v_dot2_f32_bf16 v6, v22, v22, v6\n"
      "ds_read_b64_tr_b16 v[52:53], v4 offset:16384\n"
      "ds_read_b64_tr_b16 v[54:55], v4 offset:16896\n"
      "s_waitcnt lgkmcnt(10)\n"
      "v_mfma_f32_16x16x32_bf16 v[40:43], v[56:59], v[16:19], v[40:43]\n"
      "v_dot2_f32_bf16 v5, v23, s58, v5\n"
      "v_dot2_f32_bf16 v6, v23, v23, v6\n"
      "ds_read_b64_tr_b16 v[56:57], v4 offset:34816\n"
      "ds_read_b64_tr_b16 v[58:59], v4 offset:35328\n"
      "ds_read_b64_tr_b16 v[16:17], v2 offset:16384\n"
      "ds_read_b64_tr_b16 v[18:19], v2 offset:16896\n"
      "s_waitcnt lgkmcnt(12)\n"
      "v_mfma_f32_16x16x32_bf16 v[28:31], v[60:63], v[20:23], v[28:31]\n"
      "v_dot2_f32_bf16 v5, v24, s58, v5\n"
      "v_dot2_f32_bf16 v6, v24, v24, v6\n"
      "ds_read_b64_tr_b16 v[60:61], v2 offset:34816\n"
      "ds_read_b64_tr_b16 v[62:63], v2 offset:35328\n"
      "s_waitcnt lgkmcnt(12)\n"
      "v_mfma_f32_16x16x32_bf16 v[32:35], v[12:15], v[20:23], v[32:35]\n"
      "v_dot2_f32_bf16 v5, v25, s58, v5\n"
      "v_dot2_f32_bf16 v6, v25, v25, v6\n"
      "s_waitcnt lgkmcnt(10)\n"
      "v_mfma_f32_16x16x32_bf16 v[36:39], v[8:11], v[20:23], v[36:39]\n"
      "v_dot2_f32_bf16 v5, v26, s58, v5\n"
      "v_dot2_f32_bf16 v6, v26, v26, v6\n"
      "s_waitcnt lgkmcnt(8)\n"
      "v_mfma_f32_16x16x32_bf16 v[40:43], v[48:51], v[20:23], v[40:43]\n"
      "v_dot2_f32_bf16 v5, v27, s58, v5\n"
      "v_dot2_f32_bf16 v6, v27, v27, v6\n"
      "s_waitcnt lgkmcnt(6)\n"
      "v_mfma_f32_16x16x32_bf16 v[28:31], v[52:55], v[24:27], v[28:31]\n"
      "s_nop 0\n"
      "v_permlane16_swap_b32 v5, v6\n"
      "v_add_f32 v5, v5, v6\n"
      "s_waitcnt lgkmcnt(4)\n"
      "v_mfma_f32_16x16x32_bf16 v[32:35], v[56:59], v[24:27], v[32:35]\n"
      "v_mov_b32 v6, v5\n"
      "s_waitcnt lgkmcnt(2)\n"
      "v_mfma_f32_16x16x32_bf16 v[36:39], v[16:19], v[24:27], v[36:39]\n"
      "v_permlane32_swap_b32 v5, v6\n"
      "v_add_f32 v5, v5, v6\n"
      "s_waitcnt lgkmcnt(0)\n"
      "v_mfma_f32_16x16x32_bf16 v[40:43], v[60:63], v[24:27], v[40:43]\n"
      "v_add_f32_dpp v5, v5, v5 row_ror:4 row_mask:0xf bank_mask:0xf\n"
      "s_nop 1\n"
      "v_add_f32_dpp v5, v5, v5 row_ror:8 row_mask:0xf bank_mask:0xf\n"
      "v_mov_b32 v6, v5\n"
      "s_nop 1\n"
      "v_permlane16_swap_b32 v5, v6\n"
      "v_mul_f32 v5, 0x3ae38e39, v5\n"
      "v_mul_f32 v6, 0x3ae38e39, v6\n"
      "v_fma_f32 v6, -v5, v5, v6\n"
      "v_max_f32 v6, 0, v6\n"
      "v_add_f32 v6, 0x3727c5ac, v6\n"
      "v_rsq_f32 v6, v6\n"
      "s_nop 0\n"
      "v_mul_f32 v5, v5, v6\n"
      "v_mul_f32 v44, v5, v44\n"
      "v_mul_f32 v45, v5, v45\n"
      "v_mul_f32 v46, v5, v46\n"
      "v_mul_f32 v47, v5, v47\n"
      "v_add_f32_dpp v28, v32, v28 row_shl:4 row_mask:0xf bank_mask:0xf\n"
      "v_add_f32_dpp v29, v33, v29 row_shl:4 row_mask:0xf bank_mask:0xf\n"
      "v_add_f32_dpp v30, v34, v30 row_shl:4 row_mask:0xf bank_mask:0xf\n"
      "v_add_f32_dpp v31, v35, v31 row_shl:4 row_mask:0xf bank_mask:0xf\n"
      "v_add_f32_dpp v36, v40, v36 row_shl:4 row_mask:0xf bank_mask:0xf\n"
      "v_add_f32_dpp v37, v41, v37 row_shl:4 row_mask:0xf bank_mask:0xf\n"
      "v_add_f32_dpp v38, v42, v38 row_shl:4 row_mask:0xf bank_mask:0xf\n"
      "v_add_f32_dpp v39, v43, v39 row_shl:4 row_mask:0xf bank_mask:0xf\n"
      "v_add_f32_dpp v28, v36, v28 row_shl:8 row_mask:0xf bank_mask:0xf\n"
      "v_add_f32_dpp v29, v37, v29 row_shl:8 row_mask:0xf bank_mask:0xf\n"
      "v_add_f32_dpp v30, v38, v30 row_shl:8 row_mask:0xf bank_mask:0xf\n"
      "v_add_f32_dpp v31, v39, v31 row_shl:8 row_mask:0xf bank_mask:0xf\n"
      "v_fma_f32 v28, v28, v6, -v44\n"
      "v_fma_f32 v29, v29, v6, -v45\n"
      "v_fma_f32 v30, v30, v6, -v46\n"
      "v_fma_f32 v31, v31, v6, -v47\n"
      "s_bitcmp1_b32 s48, 0\n"
      "s_cbranch_scc1 .Lup_20_%=\n"
      "v_mul_f32 v32, 0xbfb8aa3b, v28\n"
      "v_mul_f32 v33, 0xbfb8aa3b, v29\n"
      "v_mul_f32 v34, 0xbfb8aa3b, v30\n"
      "v_mul_f32 v35, 0xbfb8aa3b, v31\n"
      "v_exp_f32 v32, v32\n"
      "v_exp_f32 v33, v33\n"
      "v_exp_f32 v34, v34\n"
      "v_exp_f32 v35, v35\n"
      "v_add_f32 v32, 1.0, v32\n"
      "v_add_f32 v33, 1.0, v33\n"
      "v_add_f32 v34, 1.0, v34\n"
      "v_add_f32 v35, 1.0, v35\n"
      "v_rcp_f32 v32, v32\n"
      "v_rcp_f32 v33, v33\n"
      "v_rcp_f32 v34, v34\n"
      "v_rcp_f32 v35, v35\n"
      "v_pk_mul_f32 v[28:29], v[28:29], v[32:33]\n"
      "v_pk_mul_f32 v[30:31], v[30:31], v[34:35]\n"
      ".Lup_20_%=:\n"
      "v_cvt_pk_bf16_f32 v28, v28, v29\n"
      "v_cvt_pk_bf16_f32 v29, v30, v31\n"
      "v_min_u32 v28, -2, v28\n"
      "v_min_u32 v29, -2, v29\n"
      "s_nop 0\n"
      "v_permlane16_swap_b32 v26, v28\n"
      "v_permlane16_swap_b32 v27, v29\n"
      "s_mov_b32 exec_lo, 0x000f0000\n"
      "s_mov_b32 exec_hi, 983040\n"
      "buffer_store_dwordx4 v[26:29], v7, s[16:19], s64 offen sc1\n"
      "s_mov_b64 exec, -1\n"
      "s_branch .Lend_10_%=\n"
      ".Ldown_8_%=:\n"
      "s_cmp_eq_u32 s44, 0\n"
      "s_cbranch_scc0 .Lw0_21_%=\n"
      "v_lshlrev_b32 v1, 4, v0\n"
      "v_lshlrev_b32 v26, 2, v0\n"
      "v_add_u32 v26, 163584, v26\n"
      "s_lshl_b32 s1, s48, 12\n"
      "s_add_u32 s1, s1, s49\n"
      "s_add_u32 s1, s1, 266240\n"
      "s_lshl_b32 s64, s48, 6\n"
      "s_add_u32 s64, s64, s49\n"
      "s_add_u32 s64, s64, 561152\n"
      "v_bfe_u32 v2, v0, 1, 2\n"
      "v_mul_u32_u24 v25, 16, v2\n"
      "v_bfe_u32 v3, v0, 3, 3\n"
      "s_movk_i32 s0, 4608\n"
      "v_mad_u32_u24 v25, v3, s0, v25\n"
      "s_cmp_eq_u32 s53, 0\n"
      "s_cbranch_scc0 .Ll0_22_%=\n"
      "v_mul_u32_u24 v2, 1152, v2\n"
      "v_and_b32 v3, 1, v0\n"
      "v_lshl_add_u32 v2, v3, 3, v2\n"
      "v_lshrrev_b32 v3, 4, v0\n"
      "v_lshl_add_u32 v2, v3, 1, v2\n"
      "s_lshl_b32 s0, s48, 4\n"
      "buffer_load_ushort v24, v2, s[36:39], s0 offen\n"
      "s_branch .Lj_23_%=\n"
      ".Ll0_22_%=:\n"
      "ds_read_b32 v24, v26\n"
      ".Lj_23_%=:\n"
      "s_sleep 8\n"
      "s_mov_b32 s54, 262144\n"
      "buffer_load_dwordx4 v[8:11], v1, s[16:19], s1 offen offset:0 sc1\n"
      "buffer_load_dwordx4 v[12:15], v1, s[16:19], s1 offen offset:1024 sc1\n"
      "buffer_load_dwordx4 v[16:19], v1, s[16:19], s1 offen offset:2048 sc1\n"
      "buffer_load_dwordx4 v[20:23], v1, s[16:19], s1 offen offset:3072 sc1\n"
      "s_sleep 9\n"
      ".Llp_24_%=:\n"
      "buffer_load_dwordx4 v[28:31], v1, s[16:19], s1 offen offset:0 sc1\n"
      "buffer_load_dwordx4 v[32:35], v1, s[16:19], s1 offen offset:1024 sc1\n"
      "buffer_load_dwordx4 v[36:39], v1, s[16:19], s1 offen offset:2048 sc1\n"
      "buffer_load_dwordx4 v[40:43], v1, s[16:19], s1 offen offset:3072 sc1\n"
      "s_waitcnt vmcnt(4)\n"
      "v_max3_u32 v2, v8, v9, v10\n"
      "v_max3_u32 v3, v16, v17, v18\n"
      "v_max3_u32 v2, v2, v11, v12\n"
      "v_max3_u32 v2, v2, v13, v14\n"
      "v_max3_u32 v2, v2, v15, v19\n"
      "v_max3_u32 v2, v2, v20, v21\n"
      "v_max3_u32 v2, v2, v22, v23\n"
      "v_max_u32 v2, v2, v3\n"
      "v_cmp_eq_u32 vcc, -1, v2\n"
      "s_cbranch_vccz .Lda_25_%=\n"
      "s_sub_u32 s54, s54, 1\n"
      "s_cbranch_scc1 .Lda_25_%=\n"
      "buffer_load_dwordx4 v[8:11], v1, s[16:19], s1 offen offset:0 sc1\n"
      "buffer_load_dwordx4 v[12:15], v1, s[16:19], s1 offen offset:1024 sc1\n"
      "buffer_load_dwordx4 v[16:19], v1, s[16:19], s1 offen offset:2048 sc1\n"
      "buffer_load_dwordx4 v[20:23], v1, s[16:19], s1 offen offset:3072 sc1\n"
      "s_waitcnt vmcnt(4)\n"
      "v_max3_u32 v2, v28, v29, v30\n"
      "v_max3_u32 v3, v36, v37, v38\n"
      "v_max3_u32 v2, v2, v31, v32\n"
      "v_max3_u32 v2, v2, v33, v34\n"
      "v_max3_u32 v2, v2, v35, v39\n"
      "v_max3_u32 v2, v2, v40, v41\n"
      "v_max3_u32 v2, v2, v42, v43\n"
      "v_max_u32 v2, v2, v3\n"
      "v_cmp_eq_u32 vcc, -1, v2\n"
      "s_nop 4\n"
      "s_cbranch_vccnz .Llp_24_%=\n"
      "v_pk_add_f32 v[28:29], v[28:29], v[32:33]\n"
      "v_pk_add_f32 v[36:37], v[36:37], v[40:41]\n"
      "v_pk_add_f32 v[30:31], v[30:31], v[34:35]\n"
      "v_pk_add_f32 v[38:39], v[38:39], v[42:43]\n"
      "v_pk_add_f32 v[28:29], v[28:29], v[36:37]\n"
      "v_pk_add_f32 v[30:31], v[30:31], v[38:39]\n"
      "s_waitcnt lgkmcnt(0)\n"
      "v_permlane16_swap_b32 v28, v29\n"
      "v_permlane16_swap_b32 v30, v31\n"
      "v_lshlrev_b32 v24, 16, v24\n"
      "v_add_f32 v28, v28, v29\n"
      "v_add_f32 v30, v30, v31\n"
      "s_nop 1\n"
      "v_permlane32_swap_b32 v28, v30\n"
      "v_add_f32 v28, v28, v30\n"
      "s_nop 1\n"
      "v_add_f32_dpp v28, v28, v28 row_ror:8 row_mask:0xf bank_mask:0xf\n"
      "v_add_f32 v28, v28, v24\n"
      "v_mov_b32 v29, v28\n"
      "s_nop 1\n"
      "v_permlane16_swap_b32 v28, v29\n"
      "v_cvt_pk_bf16_f32 v30, v28, v29\n"
      "v_min_u32 v30, -2, v30\n"
      "v_mov_b32 v31, v30\n"
      "s_nop 1\n"
      "v_permlane32_swap_b32 v30, v31\n"
      "s_nop 1\n"
      "v_mov_b32_dpp v32, v30 quad_perm:[1,1,3,3] row_mask:0xf bank_mask:0xf\n"
      "v_mov_b32_dpp v33, v31 quad_perm:[1,1,3,3] row_mask:0xf bank_mask:0xf\n"
      "s_mov_b32 exec_lo, 0x55555555\n"
      "s_mov_b32 exec_hi, 0x55555555\n"
      "buffer_store_dwordx4 v[30:33], v25, s[16:19], s64 offen sc1\n"
      "s_mov_b64 exec, -1\n"
      "v_mov_b32 v34, v30\n"
      "s_nop 1\n"
      "v_mov_b32_dpp v34, v31 quad_perm:[0,1,2,3] row_mask:0xc bank_mask:0xf\n"
      "v_lshrrev_b32 v29, 16, v34\n"
      "s_nop 1\n"
      "v_mov_b32_dpp v34, v29 quad_perm:[0,1,2,3] row_mask:0xa bank_mask:0xf\n"
      "ds_write_b32 v26, v34\n"
      "s_branch .Lj_27_%=\n"
      ".Lda_25_%=:\n"
      "v_pk_add_f32 v[8:9], v[8:9], v[12:13]\n"
      "v_pk_add_f32 v[16:17], v[16:17], v[20:21]\n"
      "v_pk_add_f32 v[10:11], v[10:11], v[14:15]\n"
      "v_pk_add_f32 v[18:19], v[18:19], v[22:23]\n"
      "v_pk_add_f32 v[8:9], v[8:9], v[16:17]\n"
      "v_pk_add_f32 v[10:11], v[10:11], v[18:19]\n"
      "s_waitcnt lgkmcnt(0)\n"
      "v_permlane16_swap_b32 v8, v9\n"
      "v_permlane16_swap_b32 v10, v11\n"
      "v_lshlrev_b32 v24, 16, v24\n"
      "v_add_f32 v8, v8, v9\n"
      "v_add_f32 v10, v10, v11\n"
      "s_nop 1\n"
      "v_permlane32_swap_b32 v8, v10\n"
      "v_add_f32 v8, v8, v10\n"
      "s_nop 1\n"
      "v_add_f32_dpp v8, v8, v8 row_ror:8 row_mask:0xf bank_mask:0xf\n"
      "v_add_f32 v8, v8, v24\n"
      "v_mov_b32 v9, v8\n"
      "s_nop 1\n"
      "v_permlane16_swap_b32 v8, v9\n"
      "v_cvt_pk_bf16_f32 v10, v8, v9\n"
      "v_min_u32 v10, -2, v10\n"
      "v_mov_b32 v11, v10\n"
      "s_nop 1\n"
      "v_permlane32_swap_b32 v10, v11\n"
      "s_nop 1\n"
      "v_mov_b32_dpp v12, v10 quad_perm:[1,1,3,3] row_mask:0xf bank_mask:0xf\n"
      "v_mov_b32_dpp v13, v11 quad_perm:[1,1,3,3] row_mask:0xf bank_mask:0xf\n"
      "s_mov_b32 exec_lo, 0x55555555\n"
      "s_mov_b32 exec_hi, 0x55555555\n"
      "buffer_store_dwordx4 v[10:13], v25, s[16:19], s64 offen sc1\n"
      "s_mov_b64 exec, -1\n"
      "v_mov_b32 v14, v10\n"
      "s_nop 1\n"
      "v_mov_b32_dpp v14, v11 quad_perm:[0,1,2,3] row_mask:0xc bank_mask:0xf\n"
      "v_lshrrev_b32 v9, 16, v14\n"
      "s_nop 1\n"
      "v_mov_b32_dpp v14, v9 quad_perm:[0,1,2,3] row_mask:0xa bank_mask:0xf\n"
      "ds_write_b32 v26, v14\n"
      ".Lj_27_%=:\n"
      "s_waitcnt vmcnt(0)\n"
      ".Lw0_21_%=:\n"
      "s_cmp_eq_u32 s53, 0\n"
      "s_cbranch_scc0 .Ll0_28_%=\n"
      "s_barrier\n"
      "s_lshr_b32 s0, s33, 5\n"
      "s_and_b32 s1, s44, 1\n"
      "s_lshl_b32 s1, s1, 1\n"
      "v_lshrrev_b32 v1, 3, v0\n"
      "v_mul_u32_u24 v1, s0, v1\n"
      "v_bfe_u32 v2, v0, 4, 1\n"
      "v_or_b32 v2, s1, v2\n"
      "v_lshlrev_b32 v2, 1, v2\n"
      "v_xor_b32 v2, v0, v2\n"
      "v_and_b32 v2, 7, v2\n"
      "v_lshl_add_u32 v1, v2, 4, v1\n"
      "s_mov_b32 m0, s35\n"
      "s_add_u32 s35, s35, 4096\n"
      "buffer_load_dwordx4 v1, s[28:31], s34 offen nt lds\n"
      "s_add_u32 s34, s34, s33\n"
      "s_mov_b32 m0, s35\n"
      "s_add_u32 s35, s35, 4096\n"
      "buffer_load_dwordx4 v1, s[28:31], s34 offen nt lds\n"
      "s_add_u32 s34, s34, s33\n"
      "s_mov_b32 m0, s35\n"
      "s_add_u32 s35, s35, 4096\n"
      "buffer_load_dwordx4 v1, s[28:31], s34 offen nt lds\n"
      "s_add_u32 s34, s34, s33\n"
      "s_mov_b32 m0, s35\n"
      "s_add_u32 s35, s35, 4096\n"
      "buffer_load_dwordx4 v1, s[28:31], s34 offen nt lds\n"
      "s_add_u32 s34, s34, s33\n"
      "s_mov_b32 m0, s35\n"
      "s_add_u32 s35, s35, 4096\n"
      "buffer_load_dwordx4 v1, s[28:31], s34 offen nt lds\n"
      "s_add_u32 s34, s34, s33\n"
      "s_mov_b32 m0, s35\n"
      "s_add_u32 s35, s35, 4096\n"
      "buffer_load_dwordx4 v1, s[28:31], s34 offen nt lds\n"
      "s_add_u32 s34, s34, s33\n"
      "s_mov_b32 m0, s35\n"
      "s_add_u32 s35, s35, 4096\n"
      "buffer_load_dwordx4 v1, s[28:31], s34 offen nt lds\n"
      "s_add_u32 s34, s34, s33\n"
      "s_mov_b32 m0, s35\n"
      "s_add_u32 s35, s35, 4096\n"
      "buffer_load_dwordx4 v1, s[28:31], s34 offen nt lds\n"
      "s_add_u32 s34, s34, s33\n"
      "s_mov_b32 m0, s35\n"
      "s_add_u32 s35, s35, 4096\n"
      "buffer_load_dwordx4 v1, s[28:31], s34 offen nt lds\n"
      "s_add_u32 s34, s34, s33\n"
      "s_mov_b32 m0, s35\n"
      "s_add_u32 s35, s35, 4096\n"
      "buffer_load_dwordx4 v1, s[28:31], s34 offen nt lds\n"
      "s_add_u32 s34, s34, s33\n"
      "s_mov_b32 m0, s35\n"
      "s_add_u32 s35, s35, 4096\n"
      "buffer_load_dwordx4 v1, s[28:31], s34 offen nt lds\n"
      "s_add_u32 s34, s34, s33\n"
      "s_mov_b32 m0, s35\n"
      "s_add_u32 s35, s35, 4096\n"
      "buffer_load_dwordx4 v1, s[28:31], s34 offen nt lds\n"
      "s_add_u32 s34, s34, s33\n"
      "s_mov_b32 m0, s35\n"
      "s_add_u32 s35, s35, 4096\n"
      "buffer_load_dwordx4 v1, s[28:31], s34 offen nt lds\n"
      "s_add_u32 s34, s34, s33\n"
      "s_mov_b32 m0, s35\n"
      "s_add_u32 s35, s35, 4096\n"
      "buffer_load_dwordx4 v1, s[28:31], s34 offen nt lds\n"
      "s_add_u32 s34, s34, s33\n"
      "s_waitcnt vmcnt(0)\n"
      "s_barrier\n"
      ".Ll0_28_%=:\n"
      "v_bfe_u32 v5, v0, 2, 2\n"
      "v_lshrrev_b32 v6, 4, v0\n"
      "v_and_b32 v7, 3, v0\n"
      "v_lshl_add_u32 v3, v6, 3, v5\n"
      "v_lshlrev_b32 v3, 7, v3\n"
      "v_lshl_add_u32 v3, v7, 3, v3\n"
      "v_lshrrev_b32 v4, 1, v5\n"
      "v_and_b32 v1, 1, v6\n"
      "v_lshl_add_u32 v4, v1, 1, v4\n"
      "v_xor_b32 v4, s44, v4\n"
      "v_lshl_add_u32 v3, v4, 5, v3\n"
      "v_add_u32 v3, 65536, v3\n"
      "v_and_b32 v4, 2, v6\n"
      "v_lshlrev_b32 v4, 10, v4\n"
      "v_sub_u32 v4, v3, v4\n"
      "v_add_u32 v1, 36864, v3\n"
      "v_add_u32 v2, 36864, v4\n"
      "s_mul_i32 s0, s44, 1216\n"
      "s_add_u32 s0, s0, 157696\n"
      "v_lshlrev_b32 v28, 4, v0\n"
      "v_add_u32 v30, s0, v28\n"
      "v_lshlrev_b32 v29, 2, v0\n"
      "v_add_u32 v29, 0x400, v29\n"
      "v_add_u32 v32, s0, v29\n"
      "v_mov_b32 v33, 0x40000000\n"
      "v_cmp_gt_u32 vcc, 32, v0\n"
      "s_nop 1\n"
      "v_cndmask_b32 v29, v33, v29, vcc\n"
      "v_bfe_u32 v33, v0, 2, 2\n"
      "v_mul_u32_u24 v31, 1216, v33\n"
      "v_lshrrev_b32 v33, 4, v0\n"
      "v_lshl_add_u32 v31, v33, 6, v31\n"
      "v_and_b32 v33, 3, v0\n"
      "v_lshl_add_u32 v31, v33, 4, v31\n"
      "v_add_u32 v31, 157696, v31\n"
      "v_mov_b64 v[24:25], 0\n"
      "v_mov_b64 v[26:27], 0\n"
      "v_and_b32 v7, 3, v0\n"
      "v_lshlrev_b32 v7, 5, v7\n"
      "v_and_or_b32 v7, v0, 16, v7\n"
      "v_lshrrev_b32 v5, 5, v0\n"
      "v_lshl_add_u32 v7, v5, 10, v7\n"
      "s_and_b32 s0, s48, 7\n"
      "s_lshl2_add_u32 s1, s0, s44\n"
      "s_mul_i32 s1, s1, 1152\n"
      "s_add_u32 s1, s1, s49\n"
      "s_add_u32 s1, s1, 598016\n"
      "s_add_u32 s2, s1, 36864\n"
      "s_lshr_b32 s64, s48, 3\n"
      "s_lshl2_add_u32 s64, s64, s44\n"
      "s_lshl4_add_u32 s64, s64, s0\n"
      "s_lshl_b32 s64, s64, 7\n"
      "s_add_u32 s64, s64, s49\n"
      "s_add_u32 s64, s64, 671744\n"
      "s_mov_b32 s3, 0xffff0000\n"
      "ds_read_b64_tr_b16 v[48:49], v3 offset:0\n"
      "ds_read_b64_tr_b16 v[50:51], v3 offset:512\n"
      "ds_read_b64_tr_b16 v[52:53], v3 offset:18432\n"
      "ds_read_b64_tr_b16 v[54:55], v3 offset:18944\n"
      "ds_read_b64_tr_b16 v[56:57], v1 offset:0\n"
      "ds_read_b64_tr_b16 v[58:59], v1 offset:512\n"
      "ds_read_b64_tr_b16 v[60:61], v1 offset:18432\n"
      "ds_read_b64_tr_b16 v[62:63], v1 offset:18944\n"
      "s_cmp_eq_u32 s53, 0\n"
      "s_cbranch_scc1 .Ls_29_%=\n"
      "s_cmp_eq_u32 s44, 0\n"
      "s_cbranch_scc1 .Ls_29_%=\n"
      ".Ls_29_%=:\n"
      "s_sleep 8\n"
      "s_mov_b32 s54, 262144\n"
      "buffer_load_dwordx4 v[8:11], v28, s[16:19], s1 offen sc1\n"
      "buffer_load_dword v12, v29, s[16:19], s1 offen sc1\n"
      "buffer_load_dwordx4 v[14:17], v28, s[16:19], s2 offen sc1\n"
      "buffer_load_dword v13, v29, s[16:19], s2 offen sc1\n"
      ".Lchk_30_%=:\n"
      "s_waitcnt vmcnt(0)\n"
      "v_max3_u32 v5, v8, v9, v10\n"
      "v_max3_u32 v6, v13, v14, v15\n"
      "v_max3_u32 v5, v5, v11, v12\n"
      "v_max3_u32 v5, v5, v16, v17\n"
      "v_max_u32 v5, v5, v6\n"
      "v_cmp_eq_u32 vcc, -1, v5\n"
      "s_cbranch_vccz .Ldone_31_%=\n"
      "s_sub_u32 s54, s54, 1\n"
      "s_cbranch_scc1 .Ldone_31_%=\n"
      "buffer_load_dwordx4 v[8:11], v28, s[16:19], s1 offen sc1\n"
      "buffer_load_dword v12, v29, s[16:19], s1 offen sc1\n"
      "buffer_load_dwordx4 v[14:17], v28, s[16:19], s2 offen sc1\n"
      "buffer_load_dword v13, v29, s[16:19], s2 offen sc1\n"
      "s_branch .Lchk_30_%=\n"
      ".Ldone_31_%=:\n"
      "v_lshlrev_b32 v34, 16, v8\n"
      "v_and_b32 v35, s3, v8\n"
      "v_lshlrev_b32 v36, 16, v14\n"
      "v_and_b32 v37, s3, v14\n"
      "v_pk_mul_f32 v[34:35], v[34:35], v[36:37]\n"
      "v_cvt_pk_bf16_f32 v8, v34, v35\n"
      "v_lshlrev_b32 v38, 16, v9\n"
      "v_and_b32 v39, s3, v9\n"
      "v_lshlrev_b32 v40, 16, v15\n"
      "v_and_b32 v41, s3, v15\n"
      "v_pk_mul_f32 v[38:39], v[38:39], v[40:41]\n"
      "v_cvt_pk_bf16_f32 v9, v38, v39\n"
      "v_lshlrev_b32 v34, 16, v10\n"
      "v_and_b32 v35, s3, v10\n"
      "v_lshlrev_b32 v36, 16, v16\n"
      "v_and_b32 v37, s3, v16\n"
      "v_pk_mul_f32 v[34:35], v[34:35], v[36:37]\n"
      "v_cvt_pk_bf16_f32 v10, v34, v35\n"
      "v_lshlrev_b32 v38, 16, v11\n"
      "v_and_b32 v39, s3, v11\n"
      "v_lshlrev_b32 v40, 16, v17\n"
      "v_and_b32 v41, s3, v17\n"
      "v_pk_mul_f32 v[38:39], v[38:39], v[40:41]\n"
      "v_cvt_pk_bf16_f32 v11, v38, v39\n"
      "v_lshlrev_b32 v34, 16, v12\n"
      "v_and_b32 v35, s3, v12\n"
      "v_lshlrev_b32 v36, 16, v13\n"
      "v_and_b32 v37, s3, v13\n"
      "v_pk_mul_f32 v[34:35], v[34:35], v[36:37]\n"
      "v_cvt_pk_bf16_f32 v12, v34, v35\n"
      "ds_write_b128 v30, v[8:11]\n"
      "s_mov_b32 exec_hi, 0\n"
      "ds_write_b32 v32, v12\n"
      "s_waitcnt lgkmcnt(0)\n"
      "s_barrier\n"
      "ds_read_b128 v[24:27], v31 offset:1024\n"
      "s_mov_b64 exec, -1\n"
      "ds_read_b128 v[8:11], v31 offset:0\n"
      "ds_read_b128 v[12:15], v31 offset:256\n"
      "ds_read_b128 v[16:19], v31 offset:512\n"
      "ds_read_b128 v[20:23], v31 offset:768\n"
      "s_waitcnt lgkmcnt(3)\n"
      "v_mfma_f32_16x16x32_bf16 v[28:31], v[48:51], v[8:11], 0\n"
      "ds_read_b64_tr_b16 v[48:49], v3 offset:4096\n"
      "ds_read_b64_tr_b16 v[50:51], v3 offset:4608\n"
      "v_mfma_f32_16x16x32_bf16 v[32:35], v[52:55], v[8:11], 0\n"
      "ds_read_b64_tr_b16 v[52:53], v3 offset:22528\n"
      "ds_read_b64_tr_b16 v[54:55], v3 offset:23040\n"
      "v_mfma_f32_16x16x32_bf16 v[36:39], v[56:59], v[8:11], 0\n"
      "ds_read_b64_tr_b16 v[56:57], v1 offset:4096\n"
      "ds_read_b64_tr_b16 v[58:59], v1 offset:4608\n"
      "v_mfma_f32_16x16x32_bf16 v[40:43], v[60:63], v[8:11], 0\n"
      "ds_read_b64_tr_b16 v[60:61], v1 offset:22528\n"
      "ds_read_b64_tr_b16 v[62:63], v1 offset:23040\n"
      "ds_read_b64_tr_b16 v[8:9], v3 offset:8192\n"
      "ds_read_b64_tr_b16 v[10:11], v3 offset:8704\n"
      "s_waitcnt lgkmcnt(8)\n"
      "v_mfma_f32_16x16x32_bf16 v[28:31], v[48:51], v[12:15], v[28:31]\n"
      "ds_read_b64_tr_b16 v[48:49], v3 offset:26624\n"
      "ds_read_b64_tr_b16 v[50:51], v3 offset:27136\n"
      "s_waitcnt lgkmcnt(8)\n"
      "v_mfma_f32_16x16x32_bf16 v[32:35], v[52:55], v[12:15], v[32:35]\n"
      "ds_read_b64_tr_b16 v[52:53], v1 offset:8192\n"
      "ds_read_b64_tr_b16 v[54:55], v1 offset:8704\n"
      "s_waitcnt lgkmcnt(8)\n"
      "v_mfma_f32_16x16x32_bf16 v[36:39], v[56:59], v[12:15], v[36:39]\n"
      "ds_read_b64_tr_b16 v[56:57], v1 offset:26624\n"
      "ds_read_b64_tr_b16 v[58:59], v1 offset:27136\n"
      "s_waitcnt lgkmcnt(8)\n"
      "v_mfma_f32_16x16x32_bf16 v[40:43], v[60:63], v[12:15], v[40:43]\n"
      "ds_read_b64_tr_b16 v[60:61], v3 offset:12288\n"
      "ds_read_b64_tr_b16 v[62:63], v3 offset:12800\n"
      "ds_read_b64_tr_b16 v[12:13], v3 offset:30720\n"
      "ds_read_b64_tr_b16 v[14:15], v3 offset:31232\n"
      "s_waitcnt lgkmcnt(10)\n"
      "v_mfma_f32_16x16x32_bf16 v[28:31], v[8:11], v[16:19], v[28:31]\n"
      "ds_read_b64_tr_b16 v[8:9], v1 offset:12288\n"
      "ds_read_b64_tr_b16 v[10:11], v1 offset:12800\n"
      "s_waitcnt lgkmcnt(10)\n"
      "v_mfma_f32_16x16x32_bf16 v[32:35], v[48:51], v[16:19], v[32:35]\n"
      "ds_read_b64_tr_b16 v[48:49], v1 offset:30720\n"
      "ds_read_b64_tr_b16 v[50:51], v1 offset:31232\n"
      "s_waitcnt lgkmcnt(10)\n"
      "v_mfma_f32_16x16x32_bf16 v[36:39], v[52:55], v[16:19], v[36:39]\n"
      "ds_read_b64_tr_b16 v[52:53], v4 offset:16384\n"
      "ds_read_b64_tr_b16 v[54:55], v4 offset:16896\n"
      "s_waitcnt lgkmcnt(10)\n"
      "v_mfma_f32_16x16x32_bf16 v[40:43], v[56:59], v[16:19], v[40:43]\n"
      "ds_read_b64_tr_b16 v[56:57], v4 offset:34816\n"
      "ds_read_b64_tr_b16 v[58:59], v4 offset:35328\n"
      "ds_read_b64_tr_b16 v[16:17], v2 offset:16384\n"
      "ds_read_b64_tr_b16 v[18:19], v2 offset:16896\n"
      "s_waitcnt lgkmcnt(12)\n"
      "v_mfma_f32_16x16x32_bf16 v[28:31], v[60:63], v[20:23], v[28:31]\n"
      "ds_read_b64_tr_b16 v[60:61], v2 offset:34816\n"
      "ds_read_b64_tr_b16 v[62:63], v2 offset:35328\n"
      "s_waitcnt lgkmcnt(12)\n"
      "v_mfma_f32_16x16x32_bf16 v[32:35], v[12:15], v[20:23], v[32:35]\n"
      "s_waitcnt lgkmcnt(10)\n"
      "v_mfma_f32_16x16x32_bf16 v[36:39], v[8:11], v[20:23], v[36:39]\n"
      "s_waitcnt lgkmcnt(8)\n"
      "v_mfma_f32_16x16x32_bf16 v[40:43], v[48:51], v[20:23], v[40:43]\n"
      "s_waitcnt lgkmcnt(6)\n"
      "v_mfma_f32_16x16x32_bf16 v[28:31], v[52:55], v[24:27], v[28:31]\n"
      "s_waitcnt lgkmcnt(4)\n"
      "v_mfma_f32_16x16x32_bf16 v[32:35], v[56:59], v[24:27], v[32:35]\n"
      "s_waitcnt lgkmcnt(2)\n"
      "v_mfma_f32_16x16x32_bf16 v[36:39], v[16:19], v[24:27], v[36:39]\n"
      "s_waitcnt lgkmcnt(0)\n"
      "v_mfma_f32_16x16x32_bf16 v[40:43], v[60:63], v[24:27], v[40:43]\n"
      "s_nop 3\n"
      "v_add_f32_dpp v28, v32, v28 row_shl:4 row_mask:0xf bank_mask:0xf\n"
      "v_add_f32_dpp v29, v33, v29 row_shl:4 row_mask:0xf bank_mask:0xf\n"
      "v_add_f32_dpp v30, v34, v30 row_shl:4 row_mask:0xf bank_mask:0xf\n"
      "v_add_f32_dpp v31, v35, v31 row_shl:4 row_mask:0xf bank_mask:0xf\n"
      "v_add_f32_dpp v36, v40, v36 row_shl:4 row_mask:0xf bank_mask:0xf\n"
      "v_add_f32_dpp v37, v41, v37 row_shl:4 row_mask:0xf bank_mask:0xf\n"
      "v_add_f32_dpp v38, v42, v38 row_shl:4 row_mask:0xf bank_mask:0xf\n"
      "v_add_f32_dpp v39, v43, v39 row_shl:4 row_mask:0xf bank_mask:0xf\n"
      "v_add_f32_dpp v28, v36, v28 row_shl:8 row_mask:0xf bank_mask:0xf\n"
      "v_add_f32_dpp v29, v37, v29 row_shl:8 row_mask:0xf bank_mask:0xf\n"
      "v_add_f32_dpp v30, v38, v30 row_shl:8 row_mask:0xf bank_mask:0xf\n"
      "v_add_f32_dpp v31, v39, v31 row_shl:8 row_mask:0xf bank_mask:0xf\n"
      "s_mov_b32 exec_lo, 0x000f000f\n"
      "s_mov_b32 exec_hi, 0x000f000f\n"
      "v_min_u32 v28, -2, v28\n"
      "v_min_u32 v29, -2, v29\n"
      "v_min_u32 v30, -2, v30\n"
      "v_min_u32 v31, -2, v31\n"
      "buffer_store_dwordx4 v[28:31], v7, s[16:19], s64 offen sc1\n"
      "s_mov_b64 exec, -1\n"
      "s_cmp_eq_u32 s44, 0\n"
      "s_cbranch_scc0 .Lw0_32_%=\n"
      "v_lshlrev_b32 v1, 4, v0\n"
      "v_lshlrev_b32 v26, 2, v0\n"
      "v_add_u32 v26, 163584, v26\n"
      "ds_read_b32 v24, v26\n"
      "s_lshl_b32 s1, s48, 10\n"
      "s_add_u32 s1, s1, s49\n"
      "s_add_u32 s1, s1, 671744\n"
      "s_cmp_eq_u32 s53, 0\n"
      "s_cbranch_scc0 .Ll0_33_%=\n"
      "v_bfe_u32 v2, v0, 1, 2\n"
      "v_mul_u32_u24 v25, 16, v2\n"
      "v_bfe_u32 v3, v0, 3, 3\n"
      "s_movk_i32 s0, 4608\n"
      "v_mad_u32_u24 v25, v3, s0, v25\n"
      "s_lshl_b32 s64, s48, 6\n"
      "s_add_u32 s64, s64, 73728\n"
      "s_mov_b32 s68, s16\n"
      "s_mov_b32 s69, s17\n"
      "s_mov_b32 s70, s18\n"
      "s_mov_b32 s71, s19\n"
      "s_branch .Lj_34_%=\n"
      ".Ll0_33_%=:\n"
      "v_bfe_u32 v2, v0, 1, 2\n"
      "v_mul_u32_u24 v25, 1152, v2\n"
      "s_lshl_b32 s64, s48, 4\n"
      "v_mov_b32 v3, 0x40000000\n"
      "v_and_b32 v2, 56, v0\n"
      "v_cmp_eq_u32 vcc, 0, v2\n"
      "s_nop 1\n"
      "v_cndmask_b32 v25, v3, v25, vcc\n"
      "s_mov_b32 s68, s40\n"
      "s_mov_b32 s69, s41\n"
      "s_mov_b32 s70, s42\n"
      "s_mov_b32 s71, s43\n"
      ".Lj_34_%=:\n"
      "s_sleep 4\n"
      "s_mov_b32 s54, 262144\n"
      "buffer_load_dwordx4 v[8:11], v1, s[16:19], s1 offen sc1\n"
      "s_sleep 10\n"
      ".Llp_35_%=:\n"
      "buffer_load_dwordx4 v[32:35], v1, s[16:19], s1 offen sc1\n"
      "s_waitcnt vmcnt(1)\n"
      "v_max3_u32 v2, v8, v9, v10\n"
      "v_max_u32 v2, v2, v11\n"
      "v_cmp_eq_u32 vcc, -1, v2\n"
      "s_cbranch_vccz .Lda_36_%=\n"
      "s_sub_u32 s54, s54, 1\n"
      "s_cbranch_scc1 .Lda_36_%=\n"
      "buffer_load_dwordx4 v[8:11], v1, s[16:19], s1 offen sc1\n"
      "s_waitcnt vmcnt(1)\n"
      "v_max3_u32 v2, v32, v33, v34\n"
      "v_max_u32 v2, v2, v35\n"
      "v_cmp_eq_u32 vcc, -1, v2\n"
      "s_nop 4\n"
      "s_cbranch_vccnz .Llp_35_%=\n"
      "s_waitcnt lgkmcnt(0)\n"
      "v_permlane16_swap_b32 v32, v33\n"
      "v_permlane16_swap_b32 v34, v35\n"
      "v_lshlrev_b32 v24, 16, v24\n"
      "v_add_f32 v32, v32, v33\n"
      "v_add_f32 v34, v34, v35\n"
      "s_nop 1\n"
      "v_permlane32_swap_b32 v32, v34\n"
      "v_add_f32 v32, v32, v34\n"
      "s_nop 1\n"
      "v_add_f32_dpp v32, v32, v32 row_ror:8 row_mask:0xf bank_mask:0xf\n"
      "v_add_f32 v32, v32, v24\n"
      "v_mov_b32 v33, v32\n"
      "s_nop 1\n"
      "v_permlane16_swap_b32 v32, v33\n"
      "v_cvt_pk_bf16_f32 v34, v32, v33\n"
      "v_min_u32 v34, -2, v34\n"
      "v_mov_b32 v35, v34\n"
      "s_nop 1\n"
      "v_permlane32_swap_b32 v34, v35\n"
      "s_nop 1\n"
      "v_mov_b32_dpp v36, v34 quad_perm:[1,1,3,3] row_mask:0xf bank_mask:0xf\n"
      "v_mov_b32_dpp v37, v35 quad_perm:[1,1,3,3] row_mask:0xf bank_mask:0xf\n"
      "s_mov_b32 exec_lo, 0x55555555\n"
      "s_mov_b32 exec_hi, 0x55555555\n"
      "buffer_store_dwordx4 v[34:37], v25, s[68:71], s64 offen sc1\n"
      "s_mov_b64 exec, -1\n"
      "v_mov_b32 v38, v34\n"
      "s_nop 1\n"
      "v_mov_b32_dpp v38, v35 quad_perm:[0,1,2,3] row_mask:0xc bank_mask:0xf\n"
      "v_lshrrev_b32 v33, 16, v38\n"
      "s_nop 1\n"
      "v_mov_b32_dpp v38, v33 quad_perm:[0,1,2,3] row_mask:0xa bank_mask:0xf\n"
      "ds_write_b32 v26, v38\n"
      "s_branch .Lj_38_%=\n"
      ".Lda_36_%=:\n"
      "s_waitcnt lgkmcnt(0)\n"
      "v_permlane16_swap_b32 v8, v9\n"
      "v_permlane16_swap_b32 v10, v11\n"
      "v_lshlrev_b32 v24, 16, v24\n"
      "v_add_f32 v8, v8, v9\n"
      "v_add_f32 v10, v10, v11\n"
      "s_nop 1\n"
      "v_permlane32_swap_b32 v8, v10\n"
      "v_add_f32 v8, v8, v10\n"
      "s_nop 1\n"
      "v_add_f32_dpp v8, v8, v8 row_ror:8 row_mask:0xf bank_mask:0xf\n"
      "v_add_f32 v8, v8, v24\n"
      "v_mov_b32 v9, v8\n"
      "s_nop 1\n"
      "v_permlane16_swap_b32 v8, v9\n"
      "v_cvt_pk_bf16_f32 v10, v8, v9\n"
      "v_min_u32 v10, -2, v10\n"
      "v_mov_b32 v11, v10\n"
      "s_nop 1\n"
      "v_permlane32_swap_b32 v10, v11\n"
      "s_nop 1\n"
      "v_mov_b32_dpp v12, v10 quad_perm:[1,1,3,3] row_mask:0xf bank_mask:0xf\n"
      "v_mov_b32_dpp v13, v11 quad_perm:[1,1,3,3] row_mask:0xf bank_mask:0xf\n"
      "s_mov_b32 exec_lo, 0x55555555\n"
      "s_mov_b32 exec_hi, 0x55555555\n"
      "buffer_store_dwordx4 v[10:13], v25, s[68:71], s64 offen sc1\n"
      "s_mov_b64 exec, -1\n"
      "v_mov_b32 v14, v10\n"
      "s_nop 1\n"
      "v_mov_b32_dpp v14, v11 quad_perm:[0,1,2,3] row_mask:0xc bank_mask:0xf\n"
      "v_lshrrev_b32 v9, 16, v14\n"
      "s_nop 1\n"
      "v_mov_b32_dpp v14, v9 quad_perm:[0,1,2,3] row_mask:0xa bank_mask:0xf\n"
      "ds_write_b32 v26, v14\n"
      ".Lj_38_%=:\n"
      "s_waitcnt vmcnt(0)\n"
      ".Lw0_32_%=:\n"
      ".Lidle_9_%=:\n"
      ".Lend_10_%=:\n"
      "s_cmp_eq_u32 s53, 0\n"
      "s_cbranch_scc0 .Lexit_11_%=\n"
      "s_cmp_eq_u32 s44, 1\n"
      "s_cbranch_scc0 .Lw1_39_%=\n"
      "v_bfe_u32 v5, v0, 2, 2\n"
      "v_lshrrev_b32 v6, 4, v0\n"
      "v_and_b32 v7, 3, v0\n"
      "v_mad_u32_u24 v1, v5, 18, v6\n"
      "v_lshlrev_b32 v1, 6, v1\n"
      "v_lshl_add_u32 v1, v7, 4, v1\n"
      "v_mov_b32 v2, 0x40000000\n"
      "v_cmp_gt_u32 vcc, 32, v0\n"
      "s_nop 1\n"
      "v_cndmask_b32 v2, v2, v1, vcc\n"
      "v_lshl_add_u32 v3, v6, 3, v5\n"
      "v_lshlrev_b32 v3, 5, v3\n"
      "v_lshl_add_u32 v3, v7, 3, v3\n"
      "v_add_u32 v3, 139264, v3\n"
      "v_and_b32 v4, 2, v6\n"
      "v_lshlrev_b32 v4, 8, v4\n"
      "v_sub_u32 v4, v3, v4\n"
      "ds_read_b64_tr_b16 v[48:49], v3 offset:0\n"
      "ds_read_b64_tr_b16 v[50:51], v3 offset:128\n"
      "ds_read_b64_tr_b16 v[52:53], v3 offset:4608\n"
      "ds_read_b64_tr_b16 v[54:55], v3 offset:4736\n"
      "ds_read_b64_tr_b16 v[56:57], v3 offset:9216\n"
      "ds_read_b64_tr_b16 v[58:59], v3 offset:9344\n"
      "ds_read_b64_tr_b16 v[60:61], v3 offset:13824\n"
      "ds_read_b64_tr_b16 v[62:63], v3 offset:13952\n"
      "ds_read_b64_tr_b16 v[36:37], v3 offset:1024\n"
      "ds_read_b64_tr_b16 v[38:39], v3 offset:1152\n"
      "ds_read_b64_tr_b16 v[40:41], v3 offset:5632\n"
      "ds_read_b64_tr_b16 v[42:43], v3 offset:5760\n"
      "ds_read_b64_tr_b16 v[8:9], v3 offset:10240\n"
      "ds_read_b64_tr_b16 v[10:11], v3 offset:10368\n"
      "ds_read_b64_tr_b16 v[12:13], v3 offset:14848\n"
      "ds_read_b64_tr_b16 v[14:15], v3 offset:14976\n"
      "ds_read_b64_tr_b16 v[16:17], v3 offset:2048\n"
      "ds_read_b64_tr_b16 v[18:19], v3 offset:2176\n"
      "ds_read_b64_tr_b16 v[20:21], v3 offset:6656\n"
      "ds_read_b64_tr_b16 v[22:23], v3 offset:6784\n"
      "v_mov_b32 v7, 0\n"
      "v_cmp_gt_u32 vcc, 32, v0\n"
      "v_mov_b32 v28, s58\n"
      "v_mov_b32 v29, s58\n"
      "v_mov_b32 v30, s58\n"
      "v_mov_b32 v31, s58\n"
      "v_cndmask_b32 v32, v7, v28, vcc\n"
      "v_cndmask_b32 v33, v7, v28, vcc\n"
      "v_cndmask_b32 v34, v7, v28, vcc\n"
      "v_cndmask_b32 v35, v7, v28, vcc\n"
      "s_waitcnt lgkmcnt(15)\n"
      "v_mfma_f32_16x16x32_bf16 v[44:47], v[48:51], v[28:31], 0\n"
      "ds_read_b64_tr_b16 v[48:49], v3 offset:11264\n"
      "ds_read_b64_tr_b16 v[50:51], v3 offset:11392\n"
      "v_mfma_f32_16x16x32_bf16 v[44:47], v[52:55], v[28:31], v[44:47]\n"
      "ds_read_b64_tr_b16 v[52:53], v3 offset:15872\n"
      "ds_read_b64_tr_b16 v[54:55], v3 offset:16000\n"
      "s_waitcnt lgkmcnt(15)\n"
      "v_mfma_f32_16x16x32_bf16 v[44:47], v[56:59], v[28:31], v[44:47]\n"
      "ds_read_b64_tr_b16 v[56:57], v3 offset:3072\n"
      "ds_read_b64_tr_b16 v[58:59], v3 offset:3200\n"
      "v_mfma_f32_16x16x32_bf16 v[44:47], v[60:63], v[28:31], v[44:47]\n"
      "ds_read_b64_tr_b16 v[60:61], v3 offset:7680\n"
      "ds_read_b64_tr_b16 v[62:63], v3 offset:7808\n"
      "s_waitcnt lgkmcnt(15)\n"
      "v_mfma_f32_16x16x32_bf16 v[44:47], v[36:39], v[28:31], v[44:47]\n"
      "ds_read_b64_tr_b16 v[36:37], v3 offset:12288\n"
      "ds_read_b64_tr_b16 v[38:39], v3 offset:12416\n"
      "v_mfma_f32_16x16x32_bf16 v[44:47], v[40:43], v[28:31], v[44:47]\n"
      "ds_read_b64_tr_b16 v[40:41], v3 offset:16896\n"
      "ds_read_b64_tr_b16 v[42:43], v3 offset:17024\n"
      "s_waitcnt lgkmcnt(15)\n"
      "v_mfma_f32_16x16x32_bf16 v[44:47], v[8:11], v[28:31], v[44:47]\n"
      "ds_read_b64_tr_b16 v[8:9], v4 offset:4096\n"
      "ds_read_b64_tr_b16 v[10:11], v4 offset:4224\n"
      "v_mfma_f32_16x16x32_bf16 v[44:47], v[12:15], v[28:31], v[44:47]\n"
      "ds_read_b64_tr_b16 v[12:13], v4 offset:8704\n"
      "ds_read_b64_tr_b16 v[14:15], v4 offset:8832\n"
      "s_waitcnt lgkmcnt(15)\n"
      "v_mfma_f32_16x16x32_bf16 v[44:47], v[16:19], v[28:31], v[44:47]\n"
      "ds_read_b64_tr_b16 v[16:17], v4 offset:13312\n"
      "ds_read_b64_tr_b16 v[18:19], v4 offset:13440\n"
      "v_mfma_f32_16x16x32_bf16 v[44:47], v[20:23], v[28:31], v[44:47]\n"
      "ds_read_b64_tr_b16 v[20:21], v4 offset:17920\n"
      "ds_read_b64_tr_b16 v[22:23], v4 offset:18048\n"
      "s_waitcnt lgkmcnt(15)\n"
      "v_mfma_f32_16x16x32_bf16 v[44:47], v[48:51], v[28:31], v[44:47]\n"
      "v_mfma_f32_16x16x32_bf16 v[44:47], v[52:55], v[28:31], v[44:47]\n"
      "s_waitcnt lgkmcnt(14)\n"
      "v_mfma_f32_16x16x32_bf16 v[44:47], v[56:59], v[28:31], v[44:47]\n"
      "s_waitcnt lgkmcnt(12)\n"
      "v_mfma_f32_16x16x32_bf16 v[44:47], v[60:63], v[28:31], v[44:47]\n"
      "s_waitcnt lgkmcnt(10)\n"
      "v_mfma_f32_16x16x32_bf16 v[44:47], v[36:39], v[28:31], v[44:47]\n"
      "s_waitcnt lgkmcnt(8)\n"
      "v_mfma_f32_16x16x32_bf16 v[44:47], v[40:43], v[28:31], v[44:47]\n"
      "s_waitcnt lgkmcnt(6)\n"
      "v_mfma_f32_16x16x32_bf16 v[44:47], v[8:11], v[32:35], v[44:47]\n"
      "s_waitcnt lgkmcnt(4)\n"
      "v_mfma_f32_16x16x32_bf16 v[44:47], v[12:15], v[32:35], v[44:47]\n"
      "s_waitcnt lgkmcnt(2)\n"
      "v_mfma_f32_16x16x32_bf16 v[44:47], v[16:19], v[32:35], v[44:47]\n"
      "s_waitcnt lgkmcnt(0)\n"
      "v_mfma_f32_16x16x32_bf16 v[44:47], v[20:23], v[32:35], v[44:47]\n"
      "v_and_b32 v7, 3, v0\n"
      "v_lshlrev_b32 v7, 5, v7\n"
      "v_and_or_b32 v7, v0, 16, v7\n"
      "ds_read_b64_tr_b16 v[48:49], v3 offset:0\n"
      "ds_read_b64_tr_b16 v[50:51], v3 offset:128\n"
      "ds_read_b64_tr_b16 v[52:53], v3 offset:4608\n"
      "ds_read_b64_tr_b16 v[54:55], v3 offset:4736\n"
      "ds_read_b64_tr_b16 v[56:57], v3 offset:9216\n"
      "ds_read_b64_tr_b16 v[58:59], v3 offset:9344\n"
      "ds_read_b64_tr_b16 v[60:61], v3 offset:13824\n"
      "ds_read_b64_tr_b16 v[62:63], v3 offset:13952\n"
      "s_bfe_u32 s1, s6, 0x30000\n"
      "s_mul_i32 s1, s1, 4608\n"
      "s_add_u32 s1, s1, 73728\n"
      "s_cmp_eq_u32 s47, 1\n"
      "s_cbranch_scc1 .Ls_40_%=\n"
      "s_cmp_eq_u32 s47, 0\n"
      "s_cbranch_scc1 .Ls_40_%=\n"
      ".Ls_40_%=:\n"
      "s_sleep 8\n"
      "s_mov_b32 s54, 262144\n"
      "buffer_load_dwordx4 v[8:11], v1, s[16:19], s1 offen offset:0 sc1\n"
      "buffer_load_dwordx4 v[12:15], v1, s[16:19], s1 offen offset:256 sc1\n"
      "buffer_load_dwordx4 v[16:19], v1, s[16:19], s1 offen offset:512 sc1\n"
      "buffer_load_dwordx4 v[20:23], v1, s[16:19], s1 offen offset:768 sc1\n"
      "buffer_load_dwordx4 v[24:27], v2, s[16:19], s1 offen offset:1024 sc1\n"
      ".Lchk_41_%=:\n"
      "s_waitcnt vmcnt(0)\n"
      "v_max3_u32 v5, v8, v9, v10\n"
      "v_max3_u32 v6, v18, v19, v20\n"
      "v_max3_u32 v5, v5, v11, v12\n"
      "v_max3_u32 v5, v5, v13, v14\n"
      "v_max3_u32 v5, v5, v15, v16\n"
      "v_max3_u32 v5, v5, v17, v21\n"
      "v_max3_u32 v5, v5, v22, v23\n"
      "v_max3_u32 v5, v5, v24, v25\n"
      "v_max3_u32 v5, v5, v26, v27\n"
      "v_max_u32 v5, v5, v6\n"
      "v_cmp_eq_u32 vcc, -1, v5\n"
      "s_cbranch_vccz .Ldone_42_%=\n"
      "s_sub_u32 s54, s54, 1\n"
      "s_cbranch_scc1 .Ldone_42_%=\n"
      "buffer_load_dwordx4 v[8:11], v1, s[16:19], s1 offen offset:0 sc1\n"
      "buffer_load_dwordx4 v[12:15], v1, s[16:19], s1 offen offset:256 sc1\n"
      "buffer_load_dwordx4 v[16:19], v1, s[16:19], s1 offen offset:512 sc1\n"
      "buffer_load_dwordx4 v[20:23], v1, s[16:19], s1 offen offset:768 sc1\n"
      "buffer_load_dwordx4 v[24:27], v2, s[16:19], s1 offen offset:1024 sc1\n"
      "s_branch .Lchk_41_%=\n"
      ".Ldone_42_%=:\n"
      "v_dot2_f32_bf16 v5, v8, s58, 0\n"
      "v_dot2_f32_bf16 v6, v8, v8, 0\n"
      "v_dot2_f32_bf16 v5, v9, s58, v5\n"
      "v_dot2_f32_bf16 v6, v9, v9, v6\n"
      "v_dot2_f32_bf16 v5, v10, s58, v5\n"
      "v_dot2_f32_bf16 v6, v10, v10, v6\n"
      "v_dot2_f32_bf16 v5, v11, s58, v5\n"
      "v_dot2_f32_bf16 v6, v11, v11, v6\n"
      "s_waitcnt lgkmcnt(6)\n"
      "v_mfma_f32_16x16x32_bf16 v[28:31], v[48:51], v[8:11], 0\n"
      "v_dot2_f32_bf16 v5, v12, s58, v5\n"
      "v_dot2_f32_bf16 v6, v12, v12, v6\n"
      "ds_read_b64_tr_b16 v[48:49], v3 offset:1024\n"
      "ds_read_b64_tr_b16 v[50:51], v3 offset:1152\n"
      "s_waitcnt lgkmcnt(6)\n"
      "v_mfma_f32_16x16x32_bf16 v[32:35], v[52:55], v[8:11], 0\n"
      "v_dot2_f32_bf16 v5, v13, s58, v5\n"
      "v_dot2_f32_bf16 v6, v13, v13, v6\n"
      "ds_read_b64_tr_b16 v[52:53], v3 offset:5632\n"
      "ds_read_b64_tr_b16 v[54:55], v3 offset:5760\n"
      "s_waitcnt lgkmcnt(6)\n"
      "v_mfma_f32_16x16x32_bf16 v[36:39], v[56:59], v[8:11], 0\n"
      "v_dot2_f32_bf16 v5, v14, s58, v5\n"
      "v_dot2_f32_bf16 v6, v14, v14, v6\n"
      "ds_read_b64_tr_b16 v[56:57], v3 offset:10240\n"
      "ds_read_b64_tr_b16 v[58:59], v3 offset:10368\n"
      "s_waitcnt lgkmcnt(6)\n"
      "v_mfma_f32_16x16x32_bf16 v[40:43], v[60:63], v[8:11], 0\n"
      "v_dot2_f32_bf16 v5, v15, s58, v5\n"
      "v_dot2_f32_bf16 v6, v15, v15, v6\n"
      "ds_read_b64_tr_b16 v[60:61], v3 offset:14848\n"
      "ds_read_b64_tr_b16 v[62:63], v3 offset:14976\n"
      "ds_read_b64_tr_b16 v[8:9], v3 offset:2048\n"
      "ds_read_b64_tr_b16 v[10:11], v3 offset:2176\n"
      "s_waitcnt lgkmcnt(8)\n"
      "v_mfma_f32_16x16x32_bf16 v[28:31], v[48:51], v[12:15], v[28:31]\n"
      "v_dot2_f32_bf16 v5, v16, s58, v5\n"
      "v_dot2_f32_bf16 v6, v16, v16, v6\n"
      "ds_read_b64_tr_b16 v[48:49], v3 offset:6656\n"
      "ds_read_b64_tr_b16 v[50:51], v3 offset:6784\n"
      "s_waitcnt lgkmcnt(8)\n"
      "v_mfma_f32_16x16x32_bf16 v[32:35], v[52:55], v[12:15], v[32:35]\n"
      "v_dot2_f32_bf16 v5, v17, s58, v5\n"
      "v_dot2_f32_bf16 v6, v17, v17, v6\n"
      "ds_read_b64_tr_b16 v[52:53], v3 offset:11264\n"
      "ds_read_b64_tr_b16 v[54:55], v3 offset:11392\n"
      "s_waitcnt lgkmcnt(8)\n"
      "v_mfma_f32_16x16x32_bf16 v[36:39], v[56:59], v[12:15], v[36:39]\n"
      "v_dot2_f32_bf16 v5, v18, s58, v5\n"
      "v_dot2_f32_bf16 v6, v18, v18, v6\n"
      "ds_read_b64_tr_b16 v[56:57], v3 offset:15872\n"
      "ds_read_b64_tr_b16 v[58:59], v3 offset:16000\n"
      "s_waitcnt lgkmcnt(8)\n"
      "v_mfma_f32_16x16x32_bf16 v[40:43], v[60:63], v[12:15], v[40:43]\n"
      "v_dot2_f32_bf16 v5, v19, s58, v5\n"
      "v_dot2_f32_bf16 v6, v19, v19, v6\n"
      "ds_read_b64_tr_b16 v[60:61], v3 offset:3072\n"
      "ds_read_b64_tr_b16 v[62:63], v3 offset:3200\n"
      "ds_read_b64_tr_b16 v[12:13], v3 offset:7680\n"
      "ds_read_b64_tr_b16 v[14:15], v3 offset:7808\n"
      "s_waitcnt lgkmcnt(10)\n"
      "v_mfma_f32_16x16x32_bf16 v[28:31], v[8:11], v[16:19], v[28:31]\n"
      "v_dot2_f32_bf16 v5, v20, s58, v5\n"
      "v_dot2_f32_bf16 v6, v20, v20, v6\n"
      "ds_read_b64_tr_b16 v[8:9], v3 offset:12288\n"
      "ds_read_b64_tr_b16 v[10:11], v3 offset:12416\n"
      "s_waitcnt lgkmcnt(10)\n"
      "v_mfma_f32_16x16x32_bf16 v[32:35], v[48:51], v[16:19], v[32:35]\n"
      "v_dot2_f32_bf16 v5, v21, s58, v5\n"
      "v_dot2_f32_bf16 v6, v21, v21, v6\n"
      "ds_read_b64_tr_b16 v[48:49], v3 offset:16896\n"
      "ds_read_b64_tr_b16 v[50:51], v3 offset:17024\n"
      "s_waitcnt lgkmcnt(10)\n"
      "v_mfma_f32_16x16x32_bf16 v[36:39], v[52:55], v[16:19], v[36:39]\n"
      "v_dot2_f32_bf16 v5, v22, s58, v5\n"
      "v_dot2_f32_bf16 v6, v22, v22, v6\n"
      "ds_read_b64_tr_b16 v[52:53], v4 offset:4096\n"
      "ds_read_b64_tr_b16 v[54:55], v4 offset:4224\n"
      "s_waitcnt lgkmcnt(10)\n"
      "v_mfma_f32_16x16x32_bf16 v[40:43], v[56:59], v[16:19], v[40:43]\n"
      "v_dot2_f32_bf16 v5, v23, s58, v5\n"
      "v_dot2_f32_bf16 v6, v23, v23, v6\n"
      "ds_read_b64_tr_b16 v[56:57], v4 offset:8704\n"
      "ds_read_b64_tr_b16 v[58:59], v4 offset:8832\n"
      "ds_read_b64_tr_b16 v[16:17], v4 offset:13312\n"
      "ds_read_b64_tr_b16 v[18:19], v4 offset:13440\n"
      "s_waitcnt lgkmcnt(12)\n"
      "v_mfma_f32_16x16x32_bf16 v[28:31], v[60:63], v[20:23], v[28:31]\n"
      "v_dot2_f32_bf16 v5, v24, s58, v5\n"
      "v_dot2_f32_bf16 v6, v24, v24, v6\n"
      "ds_read_b64_tr_b16 v[60:61], v4 offset:17920\n"
      "ds_read_b64_tr_b16 v[62:63], v4 offset:18048\n"
      "s_waitcnt lgkmcnt(12)\n"
      "v_mfma_f32_16x16x32_bf16 v[32:35], v[12:15], v[20:23], v[32:35]\n"
      "v_dot2_f32_bf16 v5, v25, s58, v5\n"
      "v_dot2_f32_bf16 v6, v25, v25, v6\n"
      "s_waitcnt lgkmcnt(10)\n"
      "v_mfma_f32_16x16x32_bf16 v[36:39], v[8:11], v[20:23], v[36:39]\n"
      "v_dot2_f32_bf16 v5, v26, s58, v5\n"
      "v_dot2_f32_bf16 v6, v26, v26, v6\n"
      "s_waitcnt lgkmcnt(8)\n"
      "v_mfma_f32_16x16x32_bf16 v[40:43], v[48:51], v[20:23], v[40:43]\n"
      "v_dot2_f32_bf16 v5, v27, s58, v5\n"
      "v_dot2_f32_bf16 v6, v27, v27, v6\n"
      "s_waitcnt lgkmcnt(6)\n"
      "v_mfma_f32_16x16x32_bf16 v[28:31], v[52:55], v[24:27], v[28:31]\n"
      "s_nop 0\n"
      "v_permlane16_swap_b32 v5, v6\n"
      "v_add_f32 v5, v5, v6\n"
      "s_waitcnt lgkmcnt(4)\n"
      "v_mfma_f32_16x16x32_bf16 v[32:35], v[56:59], v[24:27], v[32:35]\n"
      "v_mov_b32 v6, v5\n"
      "s_waitcnt lgkmcnt(2)\n"
      "v_mfma_f32_16x16x32_bf16 v[36:39], v[16:19], v[24:27], v[36:39]\n"
      "v_permlane32_swap_b32 v5, v6\n"
      "v_add_f32 v5, v5, v6\n"
      "s_waitcnt lgkmcnt(0)\n"
      "v_mfma_f32_16x16x32_bf16 v[40:43], v[60:63], v[24:27], v[40:43]\n"
      "v_add_f32_dpp v5, v5, v5 row_ror:4 row_mask:0xf bank_mask:0xf\n"
      "s_nop 1\n"
      "v_add_f32_dpp v5, v5, v5 row_ror:8 row_mask:0xf bank_mask:0xf\n"
      "v_mov_b32 v6, v5\n"
      "s_nop 1\n"
      "v_permlane16_swap_b32 v5, v6\n"
      "v_mul_f32 v5, 0x3ae38e39, v5\n"
      "v_mul_f32 v6, 0x3ae38e39, v6\n"
      "v_fma_f32 v6, -v5, v5, v6\n"
      "v_max_f32 v6, 0, v6\n"
      "v_add_f32 v6, 0x3727c5ac, v6\n"
      "v_rsq_f32 v6, v6\n"
      "s_nop 0\n"
      "v_mul_f32 v5, v5, v6\n"
      "v_mul_f32 v44, v5, v44\n"
      "v_mul_f32 v45, v5, v45\n"
      "v_mul_f32 v46, v5, v46\n"
      "v_mul_f32 v47, v5, v47\n"
      "v_add_f32_dpp v28, v32, v28 row_shl:4 row_mask:0xf bank_mask:0xf\n"
      "v_add_f32_dpp v29, v33, v29 row_shl:4 row_mask:0xf bank_mask:0xf\n"
      "v_add_f32_dpp v30, v34, v30 row_shl:4 row_mask:0xf bank_mask:0xf\n"
      "v_add_f32_dpp v31, v35, v31 row_shl:4 row_mask:0xf bank_mask:0xf\n"
      "v_add_f32_dpp v36, v40, v36 row_shl:4 row_mask:0xf bank_mask:0xf\n"
      "v_add_f32_dpp v37, v41, v37 row_shl:4 row_mask:0xf bank_mask:0xf\n"
      "v_add_f32_dpp v38, v42, v38 row_shl:4 row_mask:0xf bank_mask:0xf\n"
      "v_add_f32_dpp v39, v43, v39 row_shl:4 row_mask:0xf bank_mask:0xf\n"
      "v_add_f32_dpp v28, v36, v28 row_shl:8 row_mask:0xf bank_mask:0xf\n"
      "v_add_f32_dpp v29, v37, v29 row_shl:8 row_mask:0xf bank_mask:0xf\n"
      "v_add_f32_dpp v30, v38, v30 row_shl:8 row_mask:0xf bank_mask:0xf\n"
      "v_add_f32_dpp v31, v39, v31 row_shl:8 row_mask:0xf bank_mask:0xf\n"
      "v_fma_f32 v28, v28, v6, -v44\n"
      "v_fma_f32 v29, v29, v6, -v45\n"
      "v_fma_f32 v30, v30, v6, -v46\n"
      "v_fma_f32 v31, v31, v6, -v47\n"
      "s_lshl_b32 s0, s45, 4\n"
      "s_add_u32 s0, s0, s46\n"
      "s_lshl_b32 s0, s0, 7\n"
      "s_add_u32 s0, s0, 40960\n"
      "v_cvt_pk_bf16_f32 v31, v30, v31\n"
      "v_cvt_pk_bf16_f32 v30, v28, v29\n"
      "v_min_u32 v31, -2, v31\n"
      "v_min_u32 v30, -2, v30\n"
      "s_nop 1\n"
      "v_permlane16_swap_b32 v30, v31\n"
      "s_nop 1\n"
      "v_permlane32_swap_b32 v28, v30\n"
      "v_permlane32_swap_b32 v29, v31\n"
      "s_mov_b32 exec_lo, 0\n"
      "s_mov_b32 exec_hi, 0x000f000f\n"
      "buffer_store_dwordx4 v[28:31], v7, s[16:19], s0 offen sc1\n"
      "s_mov_b64 exec, -1\n"
      ".Lw1_39_%=:\n"
      "s_waitcnt lgkmcnt(0)\n"
      "s_barrier\n"
      "v_and_b32 v5, 1, v0\n"
      "v_mov_b32 v1, 0x4444\n"
      "v_mul_u32_u24 v6, 0xaaaa, v5\n"
      "v_add_u32 v1, v1, v6\n"
      "v_lshl_add_u32 v2, v0, 4, s59\n"
      "v_bfe_u32 v6, v0, 1, 1\n"
      "v_lshrrev_b32 v7, 4, v0\n"
      "s_lshl_b32 s0, s44, 10\n"
      "s_add_u32 s0, s0, 157696\n"
      "v_lshlrev_b32 v4, 6, v5\n"
      "v_lshl_add_u32 v4, v6, 7, v4\n"
      "v_lshl_add_u32 v4, v7, 4, v4\n"
      "v_add_u32 v4, s0, v4\n"
      "v_lshlrev_b32 v3, 3, v5\n"
      "v_lshl_add_u32 v3, v6, 4, v3\n"
      "v_lshl_add_u32 v3, v7, 8, v3\n"
      "v_and_or_b32 v3, v0, 4, v3\n"
      "v_bfe_u32 v8, v0, 3, 1\n"
      "v_lshl_add_u32 v3, v8, 6, v3\n"
      "v_add_u32 v3, s0, v3\n"
      "v_lshl_add_u32 v6, v6, 3, v7\n"
      "v_lshlrev_b32 v6, 7, v6\n"
      "v_lshl_add_u32 v7, v5, 4, v6\n"
      "v_mov_b32 v9, 0x40000000\n"
      "v_cmp_eq_u32 vcc, 0, v8\n"
      "s_nop 1\n"
      "v_cndmask_b32 v5, v9, v7, vcc\n"
      "v_cndmask_b32 v6, v7, v9, vcc\n"
      "v_mov_b32 v7, 0x3e0293ee\n"
      "v_mov_b64 v[24:25], 0\n"
      "v_mov_b64 v[26:27], 0\n"
      "v_mov_b64 v[28:29], 0\n"
      "v_mov_b64 v[30:31], 0\n"
      "v_mov_b64 v[32:33], 0\n"
      "v_mov_b64 v[34:35], 0\n"
      "v_mov_b64 v[36:37], 0\n"
      "v_mov_b64 v[38:39], 0\n"
      "v_mov_b64 v[40:41], 0\n"
      "v_mov_b64 v[42:43], 0\n"
      "v_mov_b64 v[44:45], 0\n"
      "v_mov_b64 v[46:47], 0\n"
      "s_lshl_b32 s1, s45, 11\n"
      "s_lshl_b32 s0, s60, 5\n"
      "s_add_u32 s1, s1, s0\n"
      "s_add_u32 s1, s1, 40960\n"
      "s_mov_b32 s53, 1\n"
      "s_add_u32 s49, s49, 745472\n"
      "s_mov_b32 s32, 139264\n"
      "s_sleep 4\n"
      "s_mov_b32 s54, 262144\n"
      "buffer_load_dwordx4 v[8:11], v5, s[16:19], s1 offen offset:0 sc1\n"
      "buffer_load_dwordx4 v[12:15], v5, s[16:19], s1 offen offset:512 sc1\n"
      "buffer_load_dwordx4 v[16:19], v6, s[16:19], s1 offen offset:0 sc1\n"
      "buffer_load_dwordx4 v[20:23], v6, s[16:19], s1 offen offset:512 sc1\n"
      "ds_read_b128 v[48:51], v2 offset:0\n"
      "ds_read_b128 v[52:55], v2 offset:1024\n"
      "ds_read_b128 v[56:59], v2 offset:2048\n"
      ".Lchk_43_%=:\n"
      "s_waitcnt vmcnt(0)\n"
      "v_max3_u32 v60, v8, v9, v10\n"
      "v_max3_u32 v61, v16, v17, v18\n"
      "v_max3_u32 v60, v60, v11, v12\n"
      "v_max3_u32 v60, v60, v13, v14\n"
      "v_max3_u32 v60, v60, v15, v19\n"
      "v_max3_u32 v60, v60, v20, v21\n"
      "v_max3_u32 v60, v60, v22, v23\n"
      "v_max_u32 v60, v60, v61\n"
      "v_cmp_eq_u32 vcc, -1, v60\n"
      "s_cbranch_vccz .Ldone_44_%=\n"
      "s_sub_u32 s54, s54, 1\n"
      "s_cbranch_scc1 .Ldone_44_%=\n"
      "buffer_load_dwordx4 v[8:11], v5, s[16:19], s1 offen offset:0 sc1\n"
      "buffer_load_dwordx4 v[12:15], v5, s[16:19], s1 offen offset:512 sc1\n"
      "buffer_load_dwordx4 v[16:19], v6, s[16:19], s1 offen offset:0 sc1\n"
      "buffer_load_dwordx4 v[20:23], v6, s[16:19], s1 offen offset:512 sc1\n"
      "s_branch .Lchk_43_%=\n"
      ".Ldone_44_%=:\n"
      "s_mul_i32 s1, s44, 4608\n"
      "s_add_u32 s1, s1, s32\n"
      "s_movk_i32 s0, 7282\n"
      "v_add_u32 v60, 0, v0\n"
      "v_mul_u32_u24 v61, s0, v60\n"
      "v_lshrrev_b32 v61, 16, v61\n"
      "v_mul_u32_u24 v61, 1008, v61\n"
      "v_lshl_add_u32 v60, v60, 4, v61\n"
      "s_add_u32 m0, s1, 0\n"
      "s_nop 0\n"
      "buffer_load_dwordx4 v60, s[24:27], 0 offen nt lds\n"
      "v_add_u32 v60, 64, v0\n"
      "v_mul_u32_u24 v61, s0, v60\n"
      "v_lshrrev_b32 v61, 16, v61\n"
      "v_mul_u32_u24 v61, 1008, v61\n"
      "v_lshl_add_u32 v60, v60, 4, v61\n"
      "s_add_u32 m0, s1, 1024\n"
      "s_nop 0\n"
      "buffer_load_dwordx4 v60, s[24:27], 0 offen nt lds\n"
      "v_add_u32 v60, 128, v0\n"
      "v_mul_u32_u24 v61, s0, v60\n"
      "v_lshrrev_b32 v61, 16, v61\n"
      "v_mul_u32_u24 v61, 1008, v61\n"
      "v_lshl_add_u32 v60, v60, 4, v61\n"
      "s_add_u32 m0, s1, 2048\n"
      "s_nop 0\n"
      "buffer_load_dwordx4 v60, s[24:27], 0 offen nt lds\n"
      "v_add_u32 v60, 192, v0\n"
      "v_mul_u32_u24 v61, s0, v60\n"
      "v_lshrrev_b32 v61, 16, v61\n"
      "v_mul_u32_u24 v61, 1008, v61\n"
      "v_lshl_add_u32 v60, v60, 4, v61\n"
      "s_add_u32 m0, s1, 3072\n"
      "s_nop 0\n"
      "buffer_load_dwordx4 v60, s[24:27], 0 offen nt lds\n"
      "v_add_u32 v60, 256, v0\n"
      "v_mul_u32_u24 v61, s0, v60\n"
      "v_lshrrev_b32 v61, 16, v61\n"
      "v_mul_u32_u24 v61, 1008, v61\n"
      "v_lshl_add_u32 v60, v60, 4, v61\n"
      "s_add_u32 m0, s1, 4096\n"
      "s_mov_b32 exec_hi, 0\n"
      "buffer_load_dwordx4 v60, s[24:27], 0 offen nt lds\n"
      "s_mov_b64 exec, -1\n"
      "ds_read_b128 v[60:63], v2 offset:3072\n"
      "s_branch .Latt_7_%=\n"
      ".Lexit_11_%=:\n"
      "s_lshl2_add_u32 s0, s6, s44\n"
      "s_cmp_eq_u32 s0, 0\n"
      "s_cbranch_scc0 .Lx_45_%=\n"
      "v_mov_b32 v8, s50\n"
      "v_mov_b32 v9, s51\n"
      "s_mov_b64 exec, 1\n"
      "buffer_store_dwordx2 v[8:9], off, s[16:19], 0 sc1\n"
      "s_mov_b64 exec, -1\n"
      ".Lx_45_%=:\n"
      "s_waitcnt vmcnt(0) lgkmcnt(0)\n"
      : "+{v0}"(tid)
      : "{s[4:5]}"(ka), "{s6}"(wg)
      : "s0", "s1", "s2", "s3", "s7", "s8", "s9", "s10", "s11", "s12", "s13", "s14", "s15", "s16", "s17", "s18",
        "s19", "s20", "s21", "s22", "s23", "s24", "s25", "s26", "s27", "s28", "s29", "s30", "s31", "s33", "s34", "s35",
        "s36", "s37", "s38", "s39", "s40", "s41", "s42", "s43", "s44", "s45", "s46", "s47", "s48", "s49", "s50", "s51",
        "s52", "s53", "s54", "s55", "s56", "s57", "s58", "s59", "s60", "s61", "s62", "s63", "s64", "s65", "s66", "s67",
        "s68", "s69", "s70", "s71", "s72", "s73", "s74", "s75", "s76", "s77", "s78", "s79", "s80", "s81", "s82", "s83",
        "s84", "s85", "s86", "s87", "s88", "s89", "s90", "s91", "s92", "s93", "s94", "s95", "s96", "s97", "s98", "s99",
        "v1", "v2", "v3", "v4", "v5", "v6", "v7", "v8", "v9", "v10", "v11", "v12", "v13", "v14", "v15", "v16",
        "v17", "v18", "v19", "v20", "v21", "v22", "v23", "v24", "v25", "v26", "v27", "v28", "v29", "v30", "v31", "v32",
        "v33", "v34", "v35", "v36", "v37", "v38", "v39", "v40", "v41", "v42", "v43", "v44", "v45", "v46", "v47", "v48",
        "v49", "v50", "v51", "v52", "v53", "v54", "v55", "v56", "v57", "v58", "v59", "v60", "v61", "v62", "v63", "v64",
        "v65", "v66", "v67", "v68", "v69", "v70", "v71", "v72", "v73", "v74", "v75", "v76", "v77", "v78", "v79", "v80",
        "v81", "v82", "v83", "v84", "v85", "v86", "v87", "v88", "v89", "v90", "v91", "v92", "v93", "v94", "v95", "v96",
        "v97", "v98", "v99", "v100", "v101", "v102", "v103", "v104", "v105", "v106", "v107", "v108", "v109", "v110", "v111", "v112",
        "v113", "v114", "v115", "v116", "v117", "v118", "v119", "v120", "v121", "v122", "v123", "v124", "v125", "v126", "v127", "v128",
        "v129", "v130", "v131", "v132", "v133", "v134", "v135", "v136", "v137", "v138", "v139", "v140", "v141", "v142", "v143", "v144",
        "v145", "v146", "v147", "v148", "v149", "v150", "v151", "v152", "v153", "v154", "v155", "v156", "v157", "v158", "v159", "v160",
        "v161", "v162", "v163", "v164", "v165", "v166", "v167", "v168", "v169", "v170", "v171", "v172", "v173", "v174", "v175", "v176",
        "v177", "v178", "v179", "v180", "v181", "v182", "v183", "v184", "v185", "v186", "v187", "v188", "v189", "v190", "v191", "v192",
        "v193", "v194", "v195", "v196", "v197", "v198", "v199", "v200", "v201", "v202", "v203", "v204", "v205", "v206", "v207", "v208",
        "v209", "v210", "v211", "v212", "v213", "v214", "v215", "v216", "v217", "v218", "v219", "v220", "v221", "v222", "v223", "v224",
        "v225", "v226", "v227", "v228", "v229", "v230", "v231", "v232", "v233", "v234", "v235", "v236", "v237", "v238", "v239", "v240",
        "v241", "v242", "v243", "v244", "v245", "v246", "v247", "v248", "v249", "v250", "v251", "v252", "v253", "v254", "v255", "a0",
        "a1", "a2", "a3", "a4", "a5", "a6", "a7", "a8", "a9", "a10", "a11", "a12", "a13", "a14", "a15", "a16",
        "a17", "a18", "a19", "a20", "a21", "a22", "a23", "a24", "a25", "a26", "a27", "a28", "a29", "a30", "a31", "a32",
        "a33", "a34", "a35", "a36", "a37", "a38", "a39", "a40", "a41", "a42", "a43", "a44", "a45", "a46", "a47", "a48",
        "a49", "a50", "a51", "a52", "a53", "a54", "a55", "a56", "a57", "a58", "a59", "a60", "a61", "a62", "a63", "a64",
        "a65", "a66", "a67", "a68", "a69", "a70", "a71", "a72", "a73", "a74", "a75", "a76", "a77", "a78", "a79", "a80",
        "a81", "a82", "a83", "a84", "a85", "a86", "a87", "a88", "a89", "a90", "a91", "a92", "a93", "a94", "a95", "a96",
        "a97", "a98", "a99", "a100", "a101", "a102", "a103", "a104", "a105", "a106", "a107", "a108", "a109", "a110", "a111", "a112",
        "a113", "a114", "a115", "a116", "a117", "a118", "a119", "a120", "a121", "a122", "a123", "a124", "a125", "a126", "a127", "a128",
        "a129", "a130", "a131", "a132", "a133", "a134", "a135", "a136", "a137", "a138", "a139", "a140", "a141", "a142", "a143", "a144",
        "a145", "a146", "a147", "a148", "a149", "a150", "a151", "a152", "a153", "a154", "a155", "a156", "a157", "a158", "a159", "a160",
        "a161", "a162", "a163", "a164", "a165", "a166", "a167", "a168", "a169", "a170", "a171", "a172", "a173", "a174", "a175", "a176",
        "a177", "a178", "a179", "a180", "a181", "a182", "a183", "a184", "a185", "a186", "a187", "a188", "a189", "a190", "a191", "a192",
        "a193", "a194", "a195", "a196", "a197", "a198", "a199", "a200", "a201", "a202", "a203", "a204", "a205", "a206", "a207", "a208",
        "a209", "a210", "a211", "a212", "a213", "a214", "a215", "a216", "a217", "a218", "a219", "a220", "a221", "a222", "a223", "a224",
        "a225", "a226", "a227", "a228", "a229", "a230", "a231", "a232", "a233", "a234", "a235", "a236", "a237", "a238", "a239", "a240",
        "a241", "a242", "a243", "a244", "a245", "a246", "a247", "a248", "a249", "a250", "a251", "a252", "a253", "a254", "a255", "vcc",
        "scc", "memory");
}

extern "C" void kernel_launch(void* const* d_in, const int* in_sizes, int n_in, void* d_out, int out_size, void* d_ws,
                              size_t ws_size, hipStream_t stream) {
  (void)hipFuncSetAttribute((const void*)fused_decode, hipFuncAttributeMaxDynamicSharedMemorySize, 160 * 1024);
  fused_decode<<<256, 256, 160 * 1024, stream>>>(d_in[0], d_in[1], d_in[2], d_in[3], d_in[4], d_in[6], d_in[7], d_in[8],
                                                 d_out, d_ws);
}
```
